```python
import torch

def _softmax(x, axis=-1):
    return torch.softmax(x, dim=axis)
def _gelu(x):
    return torch.nn.functional.gelu(x, approximate="tanh")
def _silu(x):
    return torch.nn.functional.silu(x)
def _sigmoid(x):
    return torch.sigmoid(x)
def _layernorm(x, axis=-1, eps=1e-5):
    m = x.mean(dim=axis, keepdim=True)
    v = x.var(dim=axis, keepdim=True, unbiased=False)
    return (x - m) / torch.sqrt(v + eps)
def _scatter_add(tgt, idx, src, axis=0):
    return tgt.clone().index_add_(axis, idx, src)
def _segment_sum(src, seg, n):
    out = torch.zeros((n,) + src.shape[1:], dtype=src.dtype)
    return out.index_add_(0, seg, src)
def _pad(x, pw):
    flat = tuple(v for pair in reversed(pw) for v in pair)
    return torch.nn.functional.pad(x, flat)

kernel_name = 'concat_heads__ssm_conv_s4d__ssm_conv_s5__prompt'

def setup_inputs():

    _w = (lambda a: (lambda t: t.to(torch.bfloat16) if t.is_floating_point() else t)(torch.as_tensor(a)))
    def _A_setup():
        Bsz, L, D, N = 1, 2048, 576, 128
        return {
            "u": _w((lambda *s: torch.randn(*s, dtype=torch.bfloat16) * 0.05)(Bsz, L, D)),
            "log_A_real": _w((lambda *s: torch.randn(*s, dtype=torch.bfloat16) * 3.2)(D, N) * 0.5),
            "A_imag": _w((lambda *s: torch.randn(*s, dtype=torch.bfloat16) * 0.05)(D, N) * 3.14),
            "C_real": _w((lambda *s: torch.randn(*s, dtype=torch.bfloat16) * 0.1)(D, N) / N ** 0.5),
            "C_imag": _w((lambda *s: torch.randn(*s, dtype=torch.bfloat16) * 0.1)(D, N) / N ** 0.5),
            "log_dt": _w((lambda *s: torch.randn(*s, dtype=torch.bfloat16) * 0.05)(D) * 0.5 - 4.0),
            "D_skip": _w((lambda *s: torch.randn(*s, dtype=torch.bfloat16) * 0.05)(D)),
        }
    _A_in = _A_setup()

    def _B_setup():
        B, S, D = 1, 2048, 576
        P = D
        Lambda_re = -0.5 * (lambda _x: torch.exp(torch.as_tensor(_x)))((lambda *s: torch.randn(*s, dtype=torch.bfloat16) * 0.2)(P))
        Lambda_im = (lambda *s: torch.randn(*s, dtype=torch.bfloat16) * 0.4)(P) * 1.0
        B_re = (lambda *s: torch.randn(*s, dtype=torch.bfloat16) * 0.2)(P, D) * (D ** -0.5)
        B_im = (lambda *s: torch.randn(*s, dtype=torch.bfloat16) * 0.2)(P, D) * (D ** -0.5)
        C_re = (lambda *s: torch.randn(*s, dtype=torch.bfloat16) * 0.2)(D, P) * (P ** -0.5)
        C_im = (lambda *s: torch.randn(*s, dtype=torch.bfloat16) * 0.2)(D, P) * (P ** -0.5)
        log_dt = (lambda *s: torch.randn(*s, dtype=torch.bfloat16) * 0.2)(P) * 0.3 - 2.0
        Dskip = (lambda *s: torch.randn(*s, dtype=torch.bfloat16) * 0.05)(D)
        return {
            "x": _w((lambda *s: torch.randn(*s, dtype=torch.bfloat16) * 0.05)(B, S, D)),
            "Lambda_re": _w(Lambda_re),
            "Lambda_im": _w(Lambda_im),
            "B_re": _w(B_re),
            "B_im": _w(B_im),
            "C_re": _w(C_re),
            "C_im": _w(C_im),
            "log_dt": _w(log_dt),
            "Dskip": _w(Dskip),
        }
    _B_in = _B_setup()

    return {
        "A_u": _A_in["u"],
        "A_log_A_real": _A_in["log_A_real"],
        "A_A_imag": _A_in["A_imag"],
        "A_C_real": _A_in["C_real"],
        "A_C_imag": _A_in["C_imag"],
        "A_log_dt": _A_in["log_dt"],
        "A_D_skip": _A_in["D_skip"],
        "B_x": _B_in["x"],
        "B_Lambda_re": _B_in["Lambda_re"],
        "B_Lambda_im": _B_in["Lambda_im"],
        "B_B_re": _B_in["B_re"],
        "B_B_im": _B_in["B_im"],
        "B_C_re": _B_in["C_re"],
        "B_C_im": _B_in["C_im"],
        "B_log_dt": _B_in["log_dt"],
        "B_Dskip": _B_in["Dskip"],
    }

def reference(A_u, A_log_A_real, A_A_imag, A_C_real, A_C_imag, A_log_dt, A_D_skip, B_x, B_Lambda_re, B_Lambda_im, B_B_re, B_B_im, B_C_re, B_C_im, B_log_dt, B_Dskip):
    def _A_ref(u, log_A_real, A_imag, C_real, C_imag, log_dt, D_skip):
        L = u.shape[1]
        dt = torch.exp(log_dt)
        A = -torch.exp(log_A_real) + 1j * A_imag
        C = (C_real + 1j * C_imag) * dt[:, None]
        pos = (lambda x, dt: x.to(dt))(torch.arange(L), log_dt.dtype)
        P = torch.exp((A * dt[:, None])[:, :, None] * pos[None, None, :])
        K = 2.0 * torch.einsum("dn,dnl->dl", C, P).real
        L2 = 2 * L
        u_t = (lambda x, *d: x.permute(*d))(u, 0, 2, 1)
        Kf = (lambda x, *a, **k: torch.fft.rfft(x.float(), *a, **k))(K, L2)
        Uf = (lambda x, *a, **k: torch.fft.rfft(x.float(), *a, **k))(u_t, L2)
        y = (lambda x, *a, **k: torch.fft.irfft(x, *a, **k).to(torch.bfloat16))(Uf * Kf[None, :, :], L2)[:, :, :L]
        y = (lambda x, *d: x.permute(*d))(y, 0, 2, 1)
        return y + u * D_skip[None, None, :]

    def _B_ref(x, Lambda_re, Lambda_im, B_re, B_im, C_re, C_im, log_dt, Dskip):
        Bsz, L, H = x.shape
        Lam = Lambda_re + 1j * Lambda_im
        Bmat = B_re + 1j * B_im
        Cmat = C_re + 1j * C_im
        dt = torch.exp(log_dt)
        Lam_bar = torch.exp(dt * Lam)
        B_bar = ((Lam_bar - 1.0) / Lam)[:, None] * Bmat
        Bu = torch.einsum("bld,pd->blp", x + 0j, B_bar)
        h = torch.zeros_like(Bu[:, 0, :])
        hs = []
        for t in range(L):
            h = Lam_bar[None, :] * h + Bu[:, t, :]
            hs.append(h)
        hseq = torch.stack(hs, 1)
        y = 2.0 * torch.einsum("blp,dp->bld", hseq, Cmat).real + x * Dskip[None, None, :]
        return y

    a = _A_ref(A_u, A_log_A_real, A_A_imag, A_C_real, A_C_imag, A_log_dt, A_D_skip)
    b = _B_ref(B_x, B_Lambda_re, B_Lambda_im, B_B_re, B_B_im, B_C_re, B_C_im, B_log_dt, B_Dskip)
    return torch.concatenate([a, b], axis=-1)
```

```cpp
#include <hip/hip_runtime.h>
#include <hip/hip_bf16.h>
#include <stdint.h>
typedef unsigned short u16;
__device__ inline float bf2f(u16 v) { return __uint_as_float(((unsigned)v) << 16); }
__device__ inline u16 f2bf(float f) { unsigned u = __float_as_uint(f); u += 0x7FFF + ((u >> 16) & 1); return (u16)(u >> 16); }
__device__ inline float rbf(float f) { return bf2f(f2bf(f)); }
constexpr int L = 2048, D = 576, N = 128, P = 576;

__global__ void k_kgen(const u16* logA, const u16* Aim, const u16* Cre, const u16* Cim, const u16* logdt, float* K) {
    int d = blockIdx.x;
    __shared__ float are[N], aim[N], cre[N], cim[N];
    float dt = rbf(expf(bf2f(logdt[d])));
    if (threadIdx.x < N) {
        int n = threadIdx.x;
        are[n] = -rbf(expf(bf2f(logA[d * N + n]))) * dt;
        aim[n] = bf2f(Aim[d * N + n]) * dt;
        cre[n] = bf2f(Cre[d * N + n]) * dt;
        cim[n] = bf2f(Cim[d * N + n]) * dt;
    }
    __syncthreads();
    for (int l = threadIdx.x; l < L; l += blockDim.x) {
        float pos = rbf((float)l);
        float s = 0.f;
        for (int n = 0; n < N; n++) {
            float e = expf(are[n] * pos), sn, cs;
            sincosf(aim[n] * pos, &sn, &cs);
            s += cre[n] * e * cs - cim[n] * e * sn;
        }
        K[d * L + l] = 2.f * s;
    }
}
__global__ void k_conv(const u16* u, const float* K, const u16* Dskip, float* out) {
    int d = blockIdx.x;
    __shared__ float us[L], ks[L];
    for (int l = threadIdx.x; l < L; l += blockDim.x) { us[l] = bf2f(u[l * D + d]); ks[l] = K[d * L + l]; }
    __syncthreads();
    for (int t = threadIdx.x; t < L; t += blockDim.x) {
        float s = 0.f;
        for (int l = 0; l <= t; l++) s += ks[l] * us[t - l];
        float y = rbf(s);
        out[t * (2 * D) + d] = rbf(y + rbf(us[t] * bf2f(Dskip[d])));
    }
}
__global__ void k_bu(const u16* x, const u16* Lre, const u16* Lim, const u16* Bre, const u16* Bim, const u16* logdt, float2* Bu) {
    int l = blockIdx.x;
    __shared__ float xs[D];
    for (int i = threadIdx.x; i < D; i += blockDim.x) xs[i] = bf2f(x[l * D + i]);
    __syncthreads();
    for (int p = threadIdx.x; p < P; p += blockDim.x) {
        float sr = 0.f, si = 0.f;
        for (int k = 0; k < D; k++) { sr += xs[k] * bf2f(Bre[p * D + k]); si += xs[k] * bf2f(Bim[p * D + k]); }
        float dt = rbf(expf(bf2f(logdt[p])));
        float lr = bf2f(Lre[p]), li = bf2f(Lim[p]);
        float e = expf(dt * lr), sn, cs; sincosf(dt * li, &sn, &cs);
        float br = e * cs - 1.f, bi = e * sn;
        float den = lr * lr + li * li;
        float cr = (br * lr + bi * li) / den, ci = (bi * lr - br * li) / den;
        Bu[l * P + p] = make_float2(cr * sr - ci * si, cr * si + ci * sr);
    }
}
__global__ void k_scan(const u16* Lre, const u16* Lim, const u16* logdt, float2* Bu) {
    int p = blockIdx.x * blockDim.x + threadIdx.x;
    if (p >= P) return;
    float dt = rbf(expf(bf2f(logdt[p])));
    float lr = bf2f(Lre[p]), li = bf2f(Lim[p]);
    float e = expf(dt * lr), sn, cs; sincosf(dt * li, &sn, &cs);
    float ar = e * cs, ai = e * sn, hr = 0.f, hi = 0.f;
    for (int l = 0; l < L; l++) {
        float2 b = Bu[l * P + p];
        float nr = ar * hr - ai * hi + b.x, ni = ar * hi + ai * hr + b.y;
        hr = nr; hi = ni;
        Bu[l * P + p] = make_float2(hr, hi);
    }
}
__global__ void k_out(const u16* x, const float2* H, const u16* Cre, const u16* Cim, const u16* Dskip, float* out) {
    int l = blockIdx.x;
    __shared__ float2 hs[P];
    for (int i = threadIdx.x; i < P; i += blockDim.x) hs[i] = H[l * P + i];
    __syncthreads();
    for (int d = threadIdx.x; d < D; d += blockDim.x) {
        float s = 0.f;
        for (int p = 0; p < P; p++) s += hs[p].x * bf2f(Cre[d * P + p]) - hs[p].y * bf2f(Cim[d * P + p]);
        out[l * (2 * D) + D + d] = 2.f * s + rbf(bf2f(x[l * D + d]) * bf2f(Dskip[d]));
    }
}
extern "C" void kernel_launch(void* const* d_in, const int* in_sizes, int n_in,
                              void* d_out, int out_size, void* d_ws, size_t ws_size,
                              hipStream_t stream) {
    const u16* const* in = (const u16* const*)d_in;
    float* out = (float*)d_out;
    float* K = (float*)d_ws;
    float2* Bu = (float2*)((char*)d_ws + 8 * 1024 * 1024);
    k_kgen<<<D, 256, 0, stream>>>(in[1], in[2], in[3], in[4], in[5], K);
    k_conv<<<D, 256, 0, stream>>>(in[0], K, in[6], out);
    k_bu<<<L, 256, 0, stream>>>(in[7], in[8], in[9], in[10], in[11], in[14], Bu);
    k_scan<<<9, 64, 0, stream>>>(in[8], in[9], in[14], Bu);
    k_out<<<L, 256, 0, stream>>>(in[7], Bu, in[12], in[13], in[15], out);
}
```

```cpp
#include <hip/hip_runtime.h>
#include <stdint.h>
typedef unsigned short u16;
typedef __attribute__((ext_vector_type(8))) short bf16x8;
typedef __attribute__((ext_vector_type(4))) short s16x4;
typedef __attribute__((ext_vector_type(4))) float f32x4;
typedef __attribute__((ext_vector_type(2))) float f32x2;
typedef __attribute__((ext_vector_type(4))) unsigned u32x4;
typedef __attribute__((ext_vector_type(2))) unsigned u32x2;
#define LAS __attribute__((address_space(3)))
#define GAS __attribute__((address_space(1)))
#define DEVI __device__ __forceinline__
constexpr int L = 2048, D = 576, P = 576, OUTW = 1152;
constexpr size_t WS_E = 0, WS_FLAG = 4096, WS_SG = 65536, WS_HT = 1 << 20, WS_ST = 200u << 20;
constexpr unsigned SPIN_CAP = 1u << 20;

struct Params {
    const u16 *Au, *AlogA, *Aim, *ACre, *ACim, *Alogdt, *ADskip;
    const u16 *Bx, *BLre, *BLim, *BBre, *BBim, *BCre, *BCim, *Blogdt, *BDskip;
    float* out; char* ws;
};

DEVI float bf2f(u16 v) { return __uint_as_float(((unsigned)v) << 16); }
DEVI float rbf(float f) { return (float)(__bf16)f; }
DEVI unsigned pk2(float a, float b) {
    typedef __attribute__((ext_vector_type(2))) __bf16 bf2;
    bf2 v = {(__bf16)a, (__bf16)b};
    return __builtin_bit_cast(unsigned, v);
}
struct cf { float r, i; };
DEVI cf cmul(cf a, cf b) { return {a.r * b.r - a.i * b.i, a.r * b.i + a.i * b.r}; }
DEVI cf cfma(cf a, cf b, cf c) { return {fmaf(a.r, b.r, fmaf(-a.i, b.i, c.r)), fmaf(a.r, b.i, fmaf(a.i, b.r, c.i))}; }
DEVI void sincos_cw(float x, float& s, float& c) {
    float k = __builtin_rintf(x * 0.15915494309189535f);
    float r = fmaf(k, -6.28125f, x);
    r = fmaf(k, -1.9350051879882812e-3f, r);
    r = fmaf(k, -3.0199159819567528e-7f, r);
    float rev = r * 0.15915494309189535f;
    s = __builtin_amdgcn_sinf(rev); c = __builtin_amdgcn_cosf(rev);
}
#define BAR() asm volatile("s_waitcnt lgkmcnt(0)\n\ts_barrier" ::: "memory")
#define SBAR() do { __builtin_amdgcn_sched_barrier(0); asm volatile("s_barrier" ::: "memory"); __builtin_amdgcn_sched_barrier(0); } while (0)
#define SCHED() __builtin_amdgcn_sched_barrier(0)
#define SGB(mask, n) __builtin_amdgcn_sched_group_barrier(mask, n, 0)
#define PIN2(a) asm volatile("" : "+v"((a).r), "+v"((a).i))
#define WAITV(n) asm volatile("s_waitcnt vmcnt(%0)" :: "n"(n) : "memory")
#define STAMP(i)
#define CSTAMP(i)
#define STAMP_DECL
#define STAMP_OUT(s)

DEVI __amdgpu_buffer_rsrc_t mkrsrc(const void* ptr, unsigned bytes) { return __builtin_amdgcn_make_buffer_rsrc((void*)ptr, 0, bytes, 0x00020000); }
template <int AUX> DEVI void dma16(__amdgpu_buffer_rsrc_t r, LAS char* dst, unsigned voff, unsigned soff) {
    __builtin_amdgcn_raw_ptr_buffer_load_lds(r, (LAS void*)dst, 16, voff, soff, 0, AUX);
}
DEVI f32x4 mfma16(bf16x8 a, bf16x8 b, f32x4 c) { return __builtin_amdgcn_mfma_f32_16x16x32_bf16(a, b, c, 0, 0, 0); }
DEVI f32x2 fma2(f32x2 a, f32x2 b, f32x2 c) { return __builtin_elementwise_fma(a, b, c); }
DEVI f32x2 splat2(float a) { return (f32x2){a, a}; }
DEVI void rows4(float x, float& x0, float& x1, float& x2, float& x3) {
    const unsigned u = __float_as_uint(x);
    const auto a = __builtin_amdgcn_permlane16_swap(u, u, false, false);
    const auto b = __builtin_amdgcn_permlane32_swap(a[0], a[0], false, false);
    const auto c = __builtin_amdgcn_permlane32_swap(a[1], a[1], false, false);
    x0 = __uint_as_float(b[0]); x2 = __uint_as_float(b[1]); x1 = __uint_as_float(c[0]); x3 = __uint_as_float(c[1]);
}
DEVI float rowsum4(float x) {
    const unsigned u = __float_as_uint(x);
    const auto a = __builtin_amdgcn_permlane16_swap(u, u, false, false);
    const float s = __uint_as_float(a[0]) + __uint_as_float(a[1]);
    const unsigned v = __float_as_uint(s);
    const auto b = __builtin_amdgcn_permlane32_swap(v, v, false, false);
    return __uint_as_float(b[0]) + __uint_as_float(b[1]);
}
DEVI unsigned next_tag(unsigned e) { return e * 1664525u + 1013904223u; }

constexpr int G1_SLOT = 28672, G1_XB = 16384, G1_NS = 5;
constexpr int G2_CS = 12288, G2_NC = 10, G2_HS = 8192, G2_NH = 4, G2_H0 = G2_CS * G2_NC;
constexpr int G2_CP1 = 5;
constexpr int M_WD = 155648, M_XW = M_WD + 15 * 384, M_XP = M_XW + 4 * 384, M_TAG = M_XP + 2 * 384;
constexpr int LDS_BYTES = 163840;
static_assert(M_TAG + 16 <= LDS_BYTES, "lds");

DEVI cf csel(bool c, cf a, cf b) { cf r; r.r = c ? a.r : b.r; r.i = c ? a.i : b.i; return r; }

DEVI void b_loader(const Params& p, LAS char* lds, int xl, int j, int wave, int lane) {
    STAMP_DECL;
    STAMP(0);
    const int c = 4 * (xl >> 1) + j / 6, g = 6 * (xl & 1) + j % 6;
    const int hc = 4 * xl + j / 6, dg = j % 6;
    const int lw = wave - 4, r8 = lane >> 3, sl = lane & 7;
    {
        const __amdgpu_buffer_rsrc_t rx = mkrsrc(p.Bx, L * D * 2), rbr = mkrsrc(p.BBre, P * D * 2), rbi = mkrsrc(p.BBim, P * D * 2);
        unsigned vx[4], vb[3];
#pragma unroll
        for (int i = 0; i < 4; ++i) { const int r = 32 * lw + 8 * i + r8; vx[i] = (128 * c + r) * (D * 2) + ((sl ^ ((r >> 2) & 7)) << 4); }
#pragma unroll
        for (int i = 0; i < 3; ++i) { const int rho = 24 * lw + 8 * i + r8; const int pt = rho >> 5, pp = rho & 15;
            vb[i] = (48 * g + 16 * pt + pp) * (D * 2) + ((sl ^ ((rho >> 1) & 7)) << 4); }
        const int rot1 = ((j % 6) * 3 / 2 + 2 * (j / 6)) % 9;
        auto g1_issue = [&](int s) {
            LAS char* slot = lds + (s % G1_NS) * G1_SLOT;
            const int sg = s + rot1 >= 9 ? s + rot1 - 9 : s + rot1;
#pragma unroll
            for (int i = 0; i < 4; ++i) dma16<0>(rx, slot + (32 * lw + 8 * i) * 128, vx[i], sg * 128);
#pragma unroll
            for (int i = 0; i < 3; ++i) { const int rho0 = 24 * lw + 8 * i;
                if ((rho0 >> 4) & 1) dma16<0>(rbi, slot + G1_XB + rho0 * 128, vb[i], sg * 128);
                else dma16<0>(rbr, slot + G1_XB + rho0 * 128, vb[i], sg * 128); }
        };
#pragma unroll
        for (int s = 0; s < 5; ++s) g1_issue(s);
        STAMP(1);
        WAITV(28); BAR();
        STAMP(2);
        WAITV(21); BAR();
        WAITV(14); BAR(); g1_issue(5);
        WAITV(14); BAR(); g1_issue(6);
        WAITV(14); BAR(); g1_issue(7);
        WAITV(14); BAR(); g1_issue(8);
        WAITV(14); BAR();
        WAITV(7); BAR();
        WAITV(0); BAR();
    }
    STAMP(3);
    BAR();
    const unsigned tag = __builtin_amdgcn_readfirstlane(*(LAS unsigned*)(lds + M_TAG));
    const int rot2 = (3 * dg + (9 * (j / 6)) / 2) % 18;
    const unsigned vce = (96 * dg + r8) * (P * 2) + ((sl ^ (r8 >> 1)) << 4), vco = (96 * dg + r8) * (P * 2) + ((sl ^ (4 + (r8 >> 1))) << 4);
    auto c_piece = [&](int s, int rg) {
        const int sg = s + rot2 >= 18 ? s + rot2 - 18 : s + rot2;
        const __amdgpu_buffer_rsrc_t rc = mkrsrc(sg < 9 ? p.BCre : p.BCim, D * P * 2);
        dma16<0>(rc, lds + (s % G2_NC) * G2_CS + rg * 1024, (rg & 1) ? vco : vce, (sg < 9 ? sg : sg - 9) * 128 + rg * 8 * P * 2);
    };
    auto c_issue = [&](int s) {
#pragma unroll
        for (int i = 0; i < 3; ++i) c_piece(s, 3 * lw + i);
    };
    if (lw < 2) {
#pragma unroll
        for (int s = 0; s < G2_CP1; ++s)
#pragma unroll
            for (int i = 0; i < 6; ++i) c_piece(s, 6 * lw + i);
    } else {
        const int pl = lane & 15, q = lane >> 4;
        cf H[3];
#pragma unroll
        for (int pt = 0; pt < 3; ++pt) H[pt] = {0.f, 0.f};
        if (8 * (lw - 2) < c) {
            const __amdgpu_buffer_rsrc_t rs = mkrsrc(p.ws + WS_SG, 16 * 12 * 48 * 16);
            int cp[2]; unsigned off[2];
#pragma unroll
            for (int r = 0; r < 2; ++r) { cp[r] = c - 1 - (8 * (lw - 2) + 4 * r + q); off[r] = (((cp[r] < 0 ? 0 : cp[r]) * 12 + g) * 48 + pl) * 16; }
            u32x4 v[2][3];
            for (unsigned spins = 0; spins < SPIN_CAP; ++spins) {
                bool ok = true;
#pragma unroll
                for (int r = 0; r < 2; ++r)
#pragma unroll
                    for (int pt = 0; pt < 3; ++pt) v[r][pt] = __builtin_amdgcn_raw_buffer_load_b128(rs, off[r] + pt * 256, 0, 16);
#pragma unroll
                for (int r = 0; r < 2; ++r)
#pragma unroll
                    for (int pt = 0; pt < 3; ++pt) ok &= cp[r] < 0 || ((v[r][pt][1] == tag) & (v[r][pt][3] == tag));
                if (__all(ok)) break;
                asm volatile("" ::: "memory");
            }
#pragma unroll
            for (int r = 0; r < 2; ++r) {
                const int dl = 8 * (lw - 2) + 4 * r + q;
#pragma unroll
                for (int pt = 0; pt < 3; ++pt) {
                    const f32x2 wv = *(LAS f32x2*)(lds + M_WD + (dl < 15 ? dl : 14) * 384 + (pt * 16 + pl) * 8);
                    cf s = {__uint_as_float(v[r][pt][0]), __uint_as_float(v[r][pt][2])};
                    if (cp[r] < 0) s = {0.f, 0.f};
                    H[pt] = cfma((cf){wv.x, wv.y}, s, H[pt]);
                }
            }
#pragma unroll
            for (int pt = 0; pt < 3; ++pt) { H[pt].r = rowsum4(H[pt].r); H[pt].i = rowsum4(H[pt].i); }
        }
        if (q == 0) {
#pragma unroll
            for (int pt = 0; pt < 3; ++pt) *(LAS f32x2*)(lds + M_XP + (lw - 2) * 384 + (pt * 16 + pl) * 8) = (f32x2){H[pt].r, H[pt].i};
        }
    }
    STAMP(4);
    BAR();
    BAR();
    STAMP(5);
#pragma unroll
    for (int s = G2_CP1; s < G2_NC; ++s) c_issue(s);
    const __amdgpu_buffer_rsrc_t rh = mkrsrc(p.ws + WS_HT, 32 * 1152 * 128);
    unsigned vh[2];
#pragma unroll
    for (int i = 0; i < 2; ++i) { const int k = 16 * lw + 8 * i + r8; const int f = ((k >> 1) & 1) | (((k >> 3) & 1) << 1);
        vh[i] = (hc * 1152 + k) * 128 + ((sl ^ (f << 1)) << 4); }
    auto h_issue = [&](int s) {
        LAS char* slot = lds + G2_H0 + (s % G2_NH) * G2_HS;
        const int sg = s + rot2 >= 18 ? s + rot2 - 18 : s + rot2;
#pragma unroll
        for (int i = 0; i < 2; ++i) dma16<16>(rh, slot + (16 * lw + 8 * i) * 128, vh[i], sg * 64 * 128);
    };
    {
        GAS unsigned* fl = (GAS unsigned*)(p.ws + WS_FLAG) + ((hc >> 1) * 12 + (lane < 12 ? lane : 0)) * 32;
        for (unsigned spins = 0; spins < SPIN_CAP; ++spins) {
            const unsigned v = __hip_atomic_load(fl, __ATOMIC_RELAXED, __HIP_MEMORY_SCOPE_AGENT);
            if (__all(v == tag)) break;
        }
        asm volatile("s_waitcnt vmcnt(0)" ::: "memory");
    }
    STAMP(6);
#pragma unroll
    for (int s = 0; s < G2_NH; ++s) h_issue(s);
#define G2_AFTER(need, ngroups) [] { int n = 0; bool seen = false, seenc = (need) < G2_NC; \
            for (int q = 0; q < G2_NH; ++q) { if (seen) n += 2; if (q == (need)) seen = true; } \
            for (int t = 0; t < (ngroups); ++t) { \
                if (t + G2_NH < 18) { if (seen && seenc) n += 2; if (t + G2_NH == (need)) seen = true; } \
                if (t + G2_NC < 18) { if (seen && seenc) n += 3; if (t + G2_NC == (need)) seenc = true; } } \
            return n; }()
#define G2_STEP(s) do { constexpr int after_ = G2_AFTER((s) + 1, (s) - 1 > 0 ? (s) - 1 : 0); WAITV(after_); BAR(); \
        if ((s) >= 1) { if ((s) - 1 + G2_NH < 18) h_issue((s) - 1 + G2_NH); if ((s) - 1 + G2_NC < 18) c_issue((s) - 1 + G2_NC); } } while (0)
    { constexpr int after_ = G2_AFTER(0, 0); WAITV(after_); BAR(); }
    STAMP(7);
    G2_STEP(0); G2_STEP(1); G2_STEP(2); G2_STEP(3); G2_STEP(4); G2_STEP(5); G2_STEP(6); G2_STEP(7); G2_STEP(8);
    G2_STEP(9); G2_STEP(10); G2_STEP(11); G2_STEP(12); G2_STEP(13); G2_STEP(14); G2_STEP(15); G2_STEP(16);
    STAMP(8);
    if (wave == 4) STAMP_OUT(16);
    if (wave == 6) STAMP_OUT(32);
}

DEVI void b_compute(const Params& p, LAS char* lds, int xl, int j, int w, int lane) {
    STAMP_DECL;
    STAMP(0); CSTAMP(14);
    const int c = 4 * (xl >> 1) + j / 6, g = 6 * (xl & 1) + j % 6;
    const int hc = 4 * xl + j / 6, dg = j % 6;
    const int pl = lane & 15, q = lane >> 4;
    const unsigned eraw = __hip_atomic_load((GAS unsigned*)(p.ws + WS_E), __ATOMIC_RELAXED, __HIP_MEMORY_SCOPE_AGENT);
    cf lam[3], lam4[3], lam8[3], lam32[3], l8q[3], w8q[3], aq[3], cf2[3];
#pragma unroll
    for (int pt = 0; pt < 3; ++pt) {
        const int pp = 48 * g + 16 * pt + pl;
        const float dt = rbf(expf(bf2f(p.Blogdt[pp]))), lr = bf2f(p.BLre[pp]), li = bf2f(p.BLim[pp]);
        float sn, cs; sincos_cw(dt * li, sn, cs);
        const float e = expf(dt * lr);
        lam[pt] = {e * cs, e * sn};
        const float br = lam[pt].r - 1.f, bi = lam[pt].i, den = 1.f / (lr * lr + li * li);
        cf2[pt] = {2.f * (br * lr + bi * li) * den, 2.f * (bi * lr - br * li) * den};
        const cf l2 = cmul(lam[pt], lam[pt]);
        lam4[pt] = cmul(l2, l2); lam8[pt] = cmul(lam4[pt], lam4[pt]);
        const cf l16 = cmul(lam8[pt], lam8[pt]); lam32[pt] = cmul(l16, l16);
        const cf one = {1.f, 0.f};
        cf t = csel(q & 1, lam8[pt], one); l8q[pt] = csel(q & 2, cmul(t, l16), t);
        t = csel(q & 1, one, lam8[pt]); w8q[pt] = csel(q & 2, t, cmul(t, l16));
        const cf l64 = cmul(lam32[pt], lam32[pt]);
        t = (w & 1) ? lam32[pt] : one; t = (w & 2) ? cmul(t, l64) : t;
        aq[pt] = cmul(t, l8q[pt]);
        const cf W1 = cmul(l64, l64), W2 = cmul(W1, W1), W4 = cmul(W2, W2), W8 = cmul(W4, W4);
        t = csel(q & 1, W1, one); t = csel(q & 2, cmul(t, W2), t); t = (w & 1) ? cmul(t, W4) : t; t = (w & 2) ? cmul(t, W8) : t;
        if (4 * w + q < 15) *(LAS f32x2*)(lds + M_WD + (4 * w + q) * 384 + (pt * 16 + pl) * 8) = (f32x2){t.r, t.i};
    }
    const unsigned tag = next_tag(__builtin_amdgcn_readfirstlane(eraw));
    if (w == 0 && lane == 0) *(LAS unsigned*)(lds + M_TAG) = tag;
#pragma unroll
    for (int pt = 0; pt < 3; ++pt) { PIN2(lam[pt]); PIN2(lam4[pt]); PIN2(lam8[pt]); PIN2(lam32[pt]); PIN2(l8q[pt]); PIN2(w8q[pt]); PIN2(aq[pt]); PIN2(cf2[pt]); }
    f32x4 acc[2][3][2];
#pragma unroll
    for (int rt = 0; rt < 2; ++rt)
#pragma unroll
        for (int pt = 0; pt < 3; ++pt) { acc[rt][pt][0] = (f32x4){0, 0, 0, 0}; acc[rt][pt][1] = (f32x4){0, 0, 0, 0}; }
    unsigned aoff[2][2], boff[2];
#pragma unroll
    for (int rt = 0; rt < 2; ++rt) { const int r = 32 * w + 8 * (pl >> 2) + 4 * (pl & 1) + 2 * rt + ((pl >> 1) & 1);
#pragma unroll
        for (int kk = 0; kk < 2; ++kk) aoff[rt][kk] = r * 128 + (((4 * kk + q) ^ ((r >> 2) & 7)) << 4); }
#pragma unroll
    for (int kk = 0; kk < 2; ++kk) boff[kk] = G1_XB + pl * 128 + (((4 * kk + q) ^ ((pl >> 1) & 7)) << 4);
    STAMP(1);
    bf16x8 fa[2][2], fb[2][6];
    auto g1_read = [&](int sl, int kk) {
        LAS char* slot = lds + sl * G1_SLOT;
#pragma unroll
        for (int rt = 0; rt < 2; ++rt) fa[kk][rt] = *(LAS bf16x8*)(slot + aoff[rt][kk]);
#pragma unroll
        for (int t = 0; t < 6; ++t) fb[kk][t] = *(LAS bf16x8*)(slot + boff[kk] + t * 2048);
    };
    auto g1_mma = [&](int kk) {
#pragma unroll
        for (int rt = 0; rt < 2; ++rt)
#pragma unroll
            for (int t = 0; t < 6; ++t) acc[rt][t >> 1][t & 1] = mfma16(fa[kk][rt], fb[kk][t], acc[rt][t >> 1][t & 1]);
    };
    auto g1_sched = [&]() {
#pragma unroll
        for (int i = 0; i < 8; ++i) { SGB(0x8, 1); SGB(0x100, 1); }
        SGB(0x8, 4);
    };
    BAR();
    STAMP(2);
    g1_read(0, 0);
    int sl0 = 0;
#pragma unroll 1
    for (int s = 0; s < 8; ++s) {
        SBAR();
        const int sl1 = sl0 == G1_NS - 1 ? 0 : sl0 + 1;
        g1_read(sl0, 1);
        g1_mma(0);
        g1_sched();
        SCHED();
        g1_read(sl1, 0);
        g1_mma(1);
        g1_sched();
        sl0 = sl1;
    }
    SCHED();
    g1_read(sl0, 1);
    g1_mma(0);
    g1_sched();
    SCHED();
    g1_mma(1);
    SCHED();
    STAMP(3);
    f32x2 Lr[3][4], Li[3][4];
#pragma unroll
    for (int pt = 0; pt < 3; ++pt) { Lr[pt][0] = (f32x2){acc[0][pt][0][0], acc[0][pt][0][1]}; Li[pt][0] = (f32x2){acc[0][pt][1][0], acc[0][pt][1][1]}; }
#pragma unroll
    for (int st_ = 1; st_ < 4; ++st_) {
        f32x2 tr[3], ti[3];
#pragma unroll
        for (int pt = 0; pt < 3; ++pt) {
            const f32x2 br = {acc[st_ >> 1][pt][0][2 * (st_ & 1)], acc[st_ >> 1][pt][0][2 * (st_ & 1) + 1]};
            const f32x2 bi = {acc[st_ >> 1][pt][1][2 * (st_ & 1)], acc[st_ >> 1][pt][1][2 * (st_ & 1) + 1]};
            tr[pt] = fma2(splat2(-lam[pt].i), Li[pt][st_ - 1], br); ti[pt] = fma2(splat2(lam[pt].i), Lr[pt][st_ - 1], bi);
        }
#pragma unroll
        for (int pt = 0; pt < 3; ++pt) { Lr[pt][st_] = fma2(splat2(lam[pt].r), Lr[pt][st_ - 1], tr[pt]); Li[pt][st_] = fma2(splat2(lam[pt].r), Li[pt][st_ - 1], ti[pt]); }
    }
    cf e[3];
    {
        cf t[3];
#pragma unroll
        for (int pt = 0; pt < 3; ++pt) e[pt] = cfma(lam4[pt], (cf){Lr[pt][3].x, Li[pt][3].x}, (cf){Lr[pt][3].y, Li[pt][3].y});
#pragma unroll
        for (int pt = 0; pt < 3; ++pt) t[pt] = cmul(w8q[pt], e[pt]);
#pragma unroll
        for (int pt = 0; pt < 3; ++pt) { t[pt].r = rowsum4(t[pt].r); t[pt].i = rowsum4(t[pt].i); }
        if (q == 0) {
#pragma unroll
            for (int pt = 0; pt < 3; ++pt) *(LAS f32x2*)(lds + M_XW + w * 384 + (pt * 16 + pl) * 8) = (f32x2){t[pt].r, t[pt].i};
        }
    }
    BAR();
    if (w < 3 && c < 15) {
        const float l0r = lam32[0].r, l0i = lam32[0].i, l1r = lam32[1].r, l1i = lam32[1].i, l2r = lam32[2].r, l2i = lam32[2].i;
        cf l32; l32.r = w == 0 ? l0r : (w == 1 ? l1r : l2r); l32.i = w == 0 ? l0i : (w == 1 ? l1i : l2i);
        cf a = {0.f, 0.f};
#pragma unroll
        for (int ww = 0; ww < 4; ++ww) { const f32x2 f = *(LAS f32x2*)(lds + M_XW + ww * 384 + (w * 16 + pl) * 8); a = ww == 0 ? (cf){f.x, f.y} : cfma(l32, a, (cf){f.x, f.y}); }
        if (q == 0) {
            const __amdgpu_buffer_rsrc_t rs = mkrsrc(p.ws + WS_SG, 16 * 12 * 48 * 16);
            const u32x4 v = {__float_as_uint(a.r), tag, __float_as_uint(a.i), tag};
            __builtin_amdgcn_raw_buffer_store_b128(v, rs, ((c * 12 + g) * 48 + w * 16 + pl) * 16, 0, 16);
        }
    }
    SCHED();
    STAMP(4);
    const int dh = w & 1, lh = w >> 1;
    u32x2 xs[2][3]; u32x2 dsk[3];
#pragma unroll
    for (int dt_ = 0; dt_ < 3; ++dt_) {
        const int d0 = 96 * dg + 48 * dh + 16 * dt_ + 4 * q;
        dsk[dt_] = *(const GAS u32x2*)(p.BDskip + d0);
#pragma unroll
        for (int lt = 0; lt < 2; ++lt) xs[lt][dt_] = *(const GAS u32x2*)(p.Bx + (64 * hc + 32 * lh + 16 * lt + pl) * D + d0);
    }
    f32x2 A2r[3], A2i[3], B2r[3], B2i[3], Or_[3][4], Oi_[3][4];
    cf K[3][4];
#pragma unroll
    for (int pt = 0; pt < 3; ++pt) {
        cf e0, e1, e2, e3;
        rows4(e[pt].r, e0.r, e1.r, e2.r, e3.r); rows4(e[pt].i, e0.i, e1.i, e2.i, e3.i);
        const cf p2 = cfma(lam8[pt], e0, e1), p3 = cfma(lam8[pt], p2, e2);
        cf X; X.r = q == 0 ? 0.f : q == 1 ? e0.r : q == 2 ? p2.r : p3.r; X.i = q == 0 ? 0.f : q == 1 ? e0.i : q == 2 ? p2.i : p3.i;
        const f32x2 f0 = *(LAS f32x2*)(lds + M_XW + 0 * 384 + (pt * 16 + pl) * 8), f1 = *(LAS f32x2*)(lds + M_XW + 1 * 384 + (pt * 16 + pl) * 8), f2 = *(LAS f32x2*)(lds + M_XW + 2 * 384 + (pt * 16 + pl) * 8);
        const cf a1 = {f0.x, f0.y}, a2 = cfma(lam32[pt], a1, (cf){f1.x, f1.y}), a3 = cfma(lam32[pt], a2, (cf){f2.x, f2.y});
        cf Xw; Xw.r = w == 0 ? 0.f : w == 1 ? a1.r : w == 2 ? a2.r : a3.r; Xw.i = w == 0 ? 0.f : w == 1 ? a1.i : w == 2 ? a2.i : a3.i;
        const cf b1 = cfma(l8q[pt], Xw, X);
        const cf b2 = cfma(lam4[pt], b1, (cf){Lr[pt][3].x, Li[pt][3].x});
        const cf a2q = cmul(lam4[pt], aq[pt]);
        A2r[pt] = (f32x2){aq[pt].r, a2q.r}; A2i[pt] = (f32x2){aq[pt].i, a2q.i};
        B2r[pt] = (f32x2){b1.r, b2.r}; B2i[pt] = (f32x2){b1.i, b2.i};
        K[pt][0] = cmul(cf2[pt], lam[pt]);
#pragma unroll
        for (int st_ = 1; st_ < 4; ++st_) K[pt][st_] = cmul(K[pt][st_ - 1], lam[pt]);
#pragma unroll
        for (int st_ = 0; st_ < 4; ++st_) {
            Or_[pt][st_] = fma2(splat2(cf2[pt].r), Lr[pt][st_], splat2(-cf2[pt].i) * Li[pt][st_]);
            Oi_[pt][st_] = fma2(splat2(-cf2[pt].r), Li[pt][st_], splat2(-cf2[pt].i) * Lr[pt][st_]);
        }
    }
#pragma unroll
    for (int pt = 0; pt < 3; ++pt) {
        asm volatile("" : "+v"(A2r[pt]), "+v"(A2i[pt]), "+v"(B2r[pt]), "+v"(B2i[pt]));
#pragma unroll
        for (int st_ = 0; st_ < 4; ++st_) { asm volatile("" : "+v"(Or_[pt][st_]), "+v"(Oi_[pt][st_])); PIN2(K[pt][st_]); }
    }
    STAMP(5);
    BAR();
    STAMP(10);
    {
        const __amdgpu_buffer_rsrc_t rh = mkrsrc(p.ws + WS_HT, 32 * 1152 * 128);
        const unsigned hoff = ((2 * c + (w >> 1)) * 1152 + 48 * g + pl) * 128 + (32 * (w & 1) + 8 * q) * 2;
        f32x2 cr[3], ci[3];
#pragma unroll
        for (int pt = 0; pt < 3; ++pt) {
            const f32x2 h0 = *(LAS f32x2*)(lds + M_XP + (pt * 16 + pl) * 8), h1 = *(LAS f32x2*)(lds + M_XP + 384 + (pt * 16 + pl) * 8);
            const f32x2 Hr = splat2(h0.x + h1.x), Hi = splat2(h0.y + h1.y);
            cr[pt] = fma2(A2r[pt], Hr, fma2(-A2i[pt], Hi, B2r[pt])); ci[pt] = fma2(A2r[pt], Hi, fma2(A2i[pt], Hr, B2i[pt]));
        }
#pragma unroll
        for (int pt = 0; pt < 3; ++pt) {
            f32x2 orr[4], oi[4];
#pragma unroll
            for (int st_ = 0; st_ < 4; ++st_) {
                orr[st_] = fma2(splat2(K[pt][st_].r), cr[pt], fma2(splat2(-K[pt][st_].i), ci[pt], Or_[pt][st_]));
                oi[st_] = fma2(splat2(-K[pt][st_].r), ci[pt], fma2(splat2(-K[pt][st_].i), cr[pt], Oi_[pt][st_]));
            }
            const u32x4 vr = {pk2(orr[0].x, orr[1].x), pk2(orr[2].x, orr[3].x), pk2(orr[0].y, orr[1].y), pk2(orr[2].y, orr[3].y)};
            const u32x4 vi = {pk2(oi[0].x, oi[1].x), pk2(oi[2].x, oi[3].x), pk2(oi[0].y, oi[1].y), pk2(oi[2].y, oi[3].y)};
            __builtin_amdgcn_raw_buffer_store_b128(vr, rh, hoff + pt * 16 * 128, 0, 16);
            __builtin_amdgcn_raw_buffer_store_b128(vi, rh, hoff + (576 + pt * 16) * 128, 0, 16);
        }
    }
    STAMP(11);
    f32x4 ac2[3][2];
#pragma unroll
    for (int a = 0; a < 3; ++a)
#pragma unroll
        for (int lt = 0; lt < 2; ++lt) {
            const unsigned x0 = xs[lt][a][0], x1 = xs[lt][a][1], d0 = dsk[a][0], d1 = dsk[a][1];
            ac2[a][lt][0] = rbf(__uint_as_float(x0 << 16) * __uint_as_float(d0 << 16));
            ac2[a][lt][1] = rbf(__uint_as_float(x0 & 0xffff0000u) * __uint_as_float(d0 & 0xffff0000u));
            ac2[a][lt][2] = rbf(__uint_as_float(x1 << 16) * __uint_as_float(d1 << 16));
            ac2[a][lt][3] = rbf(__uint_as_float(x1 & 0xffff0000u) * __uint_as_float(d1 & 0xffff0000u));
            asm volatile("" : "+v"(ac2[a][lt]));
        }
    WAITV(0);
    BAR();
    if (w == 0 && lane == 0) __hip_atomic_store((GAS unsigned*)(p.ws + WS_FLAG) + (c * 12 + g) * 32, tag, __ATOMIC_RELAXED, __HIP_MEMORY_SCOPE_AGENT);
    STAMP(6);
    unsigned coff[2], hoffs[2][2];
#pragma unroll
    for (int kk = 0; kk < 2; ++kk) coff[kk] = (48 * dh + pl) * 128 + (((4 * kk + q) ^ ((pl >> 1) & 7)) << 4);
    {
        const int a = pl >> 2, b = pl & 3;
#pragma unroll
        for (int hh = 0; hh < 2; ++hh) {
            const int k = 8 * q + 4 * hh + a;
            const int f = ((k >> 1) & 1) | (((k >> 3) & 1) << 1);
#pragma unroll
            for (int lt = 0; lt < 2; ++lt) { const int ch = 2 * (2 * lh + lt) + (b >> 1);
                hoffs[lt][hh] = G2_H0 + k * 128 + ((ch ^ (f << 1)) << 4) + (b & 1) * 8; }
        }
    }
    bf16x8 fc[2][2][3], fh[2][2][2];
    auto g2_read = [&](int bf, int cslot, int hslot) {
        LAS char* cs = lds + cslot * G2_CS; LAS char* hs = lds + hslot * G2_HS;
#pragma unroll
        for (int kk = 0; kk < 2; ++kk) {
#pragma unroll
            for (int lt = 0; lt < 2; ++lt) {
                const s16x4 lo = __builtin_amdgcn_ds_read_tr16_b64_v4i16((LAS s16x4*)(hs + kk * 4096 + hoffs[lt][0]));
                const s16x4 hi = __builtin_amdgcn_ds_read_tr16_b64_v4i16((LAS s16x4*)(hs + kk * 4096 + hoffs[lt][1]));
                fh[bf][kk][lt] = __builtin_shufflevector(lo, hi, 0, 1, 2, 3, 4, 5, 6, 7);
            }
#pragma unroll
            for (int a = 0; a < 3; ++a) fc[bf][kk][a] = *(LAS bf16x8*)(cs + coff[kk] + a * 2048);
        }
    };
    auto g2_mma = [&](int bf) {
#pragma unroll
        for (int kk = 0; kk < 2; ++kk)
#pragma unroll
            for (int a = 0; a < 3; ++a)
#pragma unroll
                for (int lt = 0; lt < 2; ++lt) ac2[a][lt] = mfma16(fc[bf][kk][a], fh[bf][kk][lt], ac2[a][lt]);
    };
    auto g2_sched = [&]() {
        SGB(0x8, 1); SGB(0x100, 2); SGB(0x8, 1); SGB(0x100, 2);
#pragma unroll
        for (int i = 0; i < 10; ++i) { SGB(0x8, 1); SGB(0x100, 1); }
    };
    SBAR();
    STAMP(7);
    g2_read(0, 0, 0);
    int cs1 = 1, hs1 = 1;
#pragma unroll 1
    for (int s = 0; s < 16; s += 2) {
        SBAR();
        g2_read(1, cs1, hs1); g2_mma(0); g2_sched();
        cs1 = cs1 == G2_NC - 1 ? 0 : cs1 + 1; hs1 = hs1 == G2_NH - 1 ? 0 : hs1 + 1;
        SBAR();
        g2_read(0, cs1, hs1); g2_mma(1); g2_sched();
        cs1 = cs1 == G2_NC - 1 ? 0 : cs1 + 1; hs1 = hs1 == G2_NH - 1 ? 0 : hs1 + 1;
    }
    SBAR();
    g2_read(1, cs1, hs1); g2_mma(0); g2_sched();
    SCHED();
    g2_mma(1);
    SCHED();
    STAMP(8);
#pragma unroll
    for (int a = 0; a < 3; ++a)
#pragma unroll
        for (int lt = 0; lt < 2; ++lt)
            *(GAS f32x4*)(p.out + (64 * hc + 32 * lh + 16 * lt + pl) * OUTW + D + 96 * dg + 48 * dh + 16 * a + 4 * q) = ac2[a][lt];
    STAMP(9); CSTAMP(15);
    if (w == 0) STAMP_OUT(0);
}

constexpr int A_KSZ = 8704, A_KB = 4224, A_U0 = 9 * A_KSZ, A_USZ = 9232, A_UC = 4608, A_FLAG = A_U0 + 9 * A_USZ;
constexpr int A_ROW = 544;
static_assert(A_FLAG + 16 <= LDS_BYTES, "lds");
struct c2 { f32x2 r, i; };
DEVI c2 c2mul(c2 a, c2 b) { c2 o; o.r = fma2(a.r, b.r, -(a.i * b.i)); o.i = fma2(a.r, b.i, a.i * b.r); return o; }
DEVI c2 c2exp(f32x2 are, f32x2 arev, float pw) {
    c2 o;
#pragma unroll
    for (int h = 0; h < 2; ++h) {
        const float e = __builtin_amdgcn_exp2f(are[h] * (pw * 1.4426950408889634f));
        const float f = __builtin_amdgcn_fractf(arev[h] * pw);
        o.r[h] = e * __builtin_amdgcn_cosf(f); o.i[h] = e * __builtin_amdgcn_sinf(f);
    }
    return o;
}
template <int ROW, bool NEG> DEVI void a_putrow(unsigned m0base, c2 v) {
    const unsigned d0 = pk2(v.r.x, NEG ? -v.i.x : v.i.x), d1 = pk2(v.r.y, NEG ? -v.i.y : v.i.y);
    asm volatile("s_mov_b32 m0, %0\n\ts_nop 0\n\tds_write_addtid_b32 %1 offset:%3\n\tds_write_addtid_b32 %2 offset:%4"
                 :: "s"(m0base), "v"(d0), "v"(d1), "n"(ROW * A_ROW), "n"(ROW * A_ROW + 256) : "memory");
}
DEVI unsigned bfpair(unsigned lo16, unsigned hi16) { return __builtin_amdgcn_perm(hi16, lo16, 0x05040100u); }

DEVI void a_kgen(const Params& p, LAS char* lds, int ch, int d, int lane) {
    LAS char* kreg = lds + ch * A_KSZ;
    const unsigned m0base = __builtin_amdgcn_readfirstlane(ch * A_KSZ);
    const float dt = rbf(expf(bf2f(p.Alogdt[d])));
    f32x2 are, arev; c2 V;
#pragma unroll
    for (int h = 0; h < 2; ++h) {
        const int n = d * 128 + lane + 64 * h;
        are[h] = -rbf(expf(bf2f(p.AlogA[n]))) * dt;
        arev[h] = (bf2f(p.Aim[n]) * dt) * 0.15915494309189535f;
        V.r[h] = 2.f * (bf2f(p.ACre[n]) * dt); V.i[h] = 2.f * (bf2f(p.ACim[n]) * dt);
    }
    const c2 w1 = c2exp(are, arev, 1.f), W = c2exp(are, arev, 128.f);
    const c2 w2 = c2mul(w1, w1), w3 = c2mul(w2, w1), w4 = c2mul(w2, w2), w5 = c2mul(w4, w1), w6 = c2mul(w4, w2), w7 = c2mul(w4, w3), w8 = c2mul(w4, w4);
    const int fi = lane & 15, gq = lane >> 4;
    LAS char* frag = kreg + fi * A_ROW + gq * 16;
    bf16x8 fb[8];
    f32x4 t0 = {0, 0, 0, 0}, t1 = {0, 0, 0, 0}, t2 = {0, 0, 0, 0};
    {
        c2 y; y.r = (f32x2){1.f, 1.f}; y.i = (f32x2){0.f, 0.f};
        a_putrow<0, true>(m0base, y); y = w8;
#define LO(j) a_putrow<j, true>(m0base, y); y = c2mul(y, w8);
        LO(1) LO(2) LO(3) LO(4) LO(5) LO(6) LO(7) LO(8) LO(9) LO(10) LO(11) LO(12) LO(13) LO(14)
#undef LO
        a_putrow<15, true>(m0base, y);
#pragma unroll
        for (int ks = 0; ks < 8; ++ks) fb[ks] = *(LAS bf16x8*)(frag + ks * 64);
        asm volatile("" ::: "memory");
    }
    {
        a_putrow<0, false>(m0base, V); a_putrow<1, false>(m0base, c2mul(V, w1)); a_putrow<2, false>(m0base, c2mul(V, w2)); a_putrow<3, false>(m0base, c2mul(V, w3));
        a_putrow<4, false>(m0base, c2mul(V, w4)); a_putrow<5, false>(m0base, c2mul(V, w5)); a_putrow<6, false>(m0base, c2mul(V, w6)); a_putrow<7, false>(m0base, c2mul(V, w7));
        V = c2mul(V, W);
        a_putrow<8, false>(m0base, V); a_putrow<9, false>(m0base, c2mul(V, w1)); a_putrow<10, false>(m0base, c2mul(V, w2)); a_putrow<11, false>(m0base, c2mul(V, w3));
        a_putrow<12, false>(m0base, c2mul(V, w4)); a_putrow<13, false>(m0base, c2mul(V, w5)); a_putrow<14, false>(m0base, c2mul(V, w6)); a_putrow<15, false>(m0base, c2mul(V, w7));
#pragma unroll
        for (int ks = 0; ks < 8; ++ks) t0 = mfma16(*(LAS bf16x8*)(frag + ks * 64), fb[ks], t0);
        asm volatile("" ::: "memory");
    }
    {
        V = c2mul(V, W);
        a_putrow<0, false>(m0base, V); a_putrow<1, false>(m0base, c2mul(V, w2)); a_putrow<2, false>(m0base, c2mul(V, w4)); a_putrow<3, false>(m0base, c2mul(V, w6));
        V = c2mul(V, W);
        a_putrow<4, false>(m0base, V); a_putrow<5, false>(m0base, c2mul(V, w2)); a_putrow<6, false>(m0base, c2mul(V, w4)); a_putrow<7, false>(m0base, c2mul(V, w6));
        V = c2mul(V, W); a_putrow<8, false>(m0base, V); a_putrow<9, false>(m0base, c2mul(V, w4));
        V = c2mul(V, W); a_putrow<10, false>(m0base, V); a_putrow<11, false>(m0base, c2mul(V, w4));
        V = c2mul(V, W); a_putrow<12, false>(m0base, V); a_putrow<13, false>(m0base, c2mul(V, w4));
        V = c2mul(V, W); a_putrow<14, false>(m0base, V); a_putrow<15, false>(m0base, c2mul(V, w4));
#pragma unroll
        for (int ks = 0; ks < 8; ++ks) t1 = mfma16(*(LAS bf16x8*)(frag + ks * 64), fb[ks], t1);
        asm volatile("" ::: "memory");
    }
    {
        V = c2mul(V, W); a_putrow<0, false>(m0base, V); V = c2mul(V, W); a_putrow<1, false>(m0base, V);
        V = c2mul(V, W); a_putrow<2, false>(m0base, V); V = c2mul(V, W); a_putrow<3, false>(m0base, V);
        V = c2mul(V, W); a_putrow<4, false>(m0base, V); V = c2mul(V, W); a_putrow<5, false>(m0base, V);
        V = c2mul(V, W); a_putrow<6, false>(m0base, V); V = c2mul(V, W); a_putrow<7, false>(m0base, V);
        V = c2mul(V, W); a_putrow<8, false>(m0base, V);
#pragma unroll
        for (int ks = 0; ks < 8; ++ks) t2 = mfma16(*(LAS bf16x8*)(frag + ks * 64), fb[ks], t2);
        asm volatile("" ::: "memory");
    }
    LAS char* tc = kreg;
    *(LAS u32x2*)(tc + 2 * (128 * (gq >> 1) + 4 * (gq & 1) + 8 * fi)) = (u32x2){pk2(t0[0], t0[1]), pk2(t0[2], t0[3])};
    if (gq < 2) *(LAS u32x2*)(tc + 2 * (256 + 64 * gq + 4 * fi)) = (u32x2){pk2(t1[0], t1[1]), pk2(t1[2], t1[3])};
    else { *(LAS unsigned*)(tc + 2 * (384 + 64 * (gq - 2) + 2 * fi)) = pk2(t1[0], t1[1]); *(LAS unsigned*)(tc + 2 * (384 + 64 * (gq - 2) + 32 + 2 * fi)) = pk2(t1[2], t1[3]); }
    if (gq < 2) {
#pragma unroll
        for (int r = 0; r < 4; ++r) *(LAS u16*)(tc + 2 * (512 + 16 * (4 * gq + r) + fi)) = (u16)pk2(t2[r], 0.f);
    } else if (lane == 32) *(LAS u16*)(tc + 2 * 640) = (u16)pk2(t2[0], 0.f);
    asm volatile("" ::: "memory");
    const unsigned eo3 = *(LAS unsigned*)(tc + 2 * (512 + 2 * lane)), n3 = *(LAS u16*)(tc + 2 * (512 + 2 * lane + 2));
    const unsigned eo2 = *(LAS unsigned*)(tc + 2 * (384 + 2 * lane)), n2 = *(LAS u16*)(tc + 2 * (384 + 2 * lane + 2));
    const unsigned eo1 = *(LAS unsigned*)(tc + 2 * (256 + 2 * lane)), n1 = *(LAS u16*)(tc + 2 * (256 + 2 * lane + 2));
    const u32x2 q0 = *(LAS u32x2*)(tc + 8 * lane);
    unsigned m0v = *(LAS u16*)(tc + 8 * lane - (lane ? 2 : 0));
    const unsigned t255 = *(LAS u16*)(tc + 2 * 255);
    asm volatile("" ::: "memory");
    if (lane == 0) m0v = 0;
    LAS char* ka = kreg; LAS char* kb = kreg + A_KB;
    {
        const unsigned E = eo3 & 0xffffu, O = eo3 >> 16, N = n3;
        const unsigned EE = bfpair(E, E), OO = bfpair(O, O), NN = bfpair(N, N);
        const int o = 2 * (2032 - 16 * (64 + lane));
        *(LAS u32x4*)(kb + o) = (u32x4){NN, NN, OO, OO}; *(LAS u32x4*)(kb + o + 16) = (u32x4){OO, bfpair(O, E), EE, EE};
        *(LAS u32x4*)(ka + o) = (u32x4){NN, bfpair(N, O), OO, OO}; *(LAS u32x4*)(ka + o + 16) = (u32x4){OO, EE, EE, EE};
    }
    {
        const unsigned E = eo2 & 0xffffu, O = eo2 >> 16, N = n2;
        const unsigned EE = bfpair(E, E), OO = bfpair(O, O);
        const int o = 2 * (2040 - 8 * (64 + lane));
        *(LAS u32x4*)(kb + o) = (u32x4){bfpair(N, N), OO, bfpair(O, E), EE};
        *(LAS u32x4*)(ka + o) = (u32x4){bfpair(N, O), OO, EE, EE};
    }
    {
        const unsigned E = eo1 & 0xffffu, O = eo1 >> 16, N = n1, Em = lane == 0 ? t255 : E;
        const int o = 2 * (2044 - 4 * (64 + lane));
        *(LAS u32x2*)(kb + o) = (u32x2){bfpair(N, O), bfpair(E, E)};
        *(LAS u32x2*)(ka + o) = (u32x2){bfpair(O, E), bfpair(E, Em)};
    }
    {
        const unsigned T0 = q0[0] & 0xffffu, T1 = q0[0] >> 16, T2 = q0[1] & 0xffffu, T3 = q0[1] >> 16;
        const int o = 2 * (2044 - 4 * lane);
        *(LAS u32x2*)(kb + o) = (u32x2){bfpair(T3, T2), bfpair(T1, T0)};
        *(LAS u32x2*)(ka + o) = (u32x2){bfpair(T2, T1), bfpair(T0, m0v)};
    }
    if (lane < 7) { *(LAS u32x2*)(kb + 4096 + 8 * lane) = (u32x2){0u, 0u}; *(LAS u32x2*)(ka + 4096 + 8 * lane) = (u32x2){0u, 0u}; }
}

template <int TM> DEVI void a_conv(LAS char* lds, int ch, int lane, f32x4 (&acc)[8]) {
    constexpr int TMAX = TM & 128 ? 7 : TM & 64 ? 6 : TM & 32 ? 5 : 4;
    const int fi = lane & 15, gq = lane >> 4, par = fi & 1, hp = fi >> 1;
    LAS char* kp = lds + ch * A_KSZ + par * A_KB + 2 * (2044 - 4 * hp + 8 * gq) - 512 * 7;
    LAS char* ub = lds + A_U0 + ch * A_USZ + (par ? 0 : A_UC);
    bf16x8 fa[2][8], fu[2][8];
    auto rd = [&](int bf, int nu) {
        LAS char* k = kp - 64 * nu;
        const int R = hp - nu + 7;
        const unsigned un = (2 * gq) ^ (((R >> 2) & 1) << 2) ^ (par ? 0 : 1);
        LAS char* u0 = ub + 64 * R + 8 * un; LAS char* u1 = ub + 64 * R + 8 * (un ^ 1);
#pragma unroll
        for (int m = 0; m <= TMAX; ++m) {
            const u32x2 alo = *(volatile LAS u32x2*)(k + 512 * (7 - m)), ahi = *(volatile LAS u32x2*)(k + 512 * (7 - m) + 8);
            fa[bf][m] = __builtin_bit_cast(bf16x8, (u32x4){alo[0], alo[1], ahi[0], ahi[1]});
            bool need = false;
#pragma unroll
            for (int t = m; t < 8; ++t) need |= ((TM >> t) & 1) != 0;
            const u32x2 blo = *(volatile LAS u32x2*)(u0 + 512 * m), bhi = *(volatile LAS u32x2*)(u1 + 512 * m);
            fu[bf][m] = __builtin_bit_cast(bf16x8, (u32x4){blo[0], blo[1], bhi[0], bhi[1]});
        }
    };
    auto mm = [&](int bf) {
#pragma unroll
        for (int dg_ = 0; dg_ <= 2 * TMAX; ++dg_)
#pragma unroll
            for (int m = 0; m <= TMAX; ++m) {
                const int kp_ = dg_ - m;
                if (kp_ < 0 || kp_ > TMAX || m + kp_ > 7) continue;
                if (m > kp_ && false) continue;
                const int t = m + kp_;
                if (t == dg_ && ((TM >> t) & 1)) acc[t] = mfma16(fa[bf][m], fu[bf][kp_], acc[t]);
            }
    };
    auto sched = [&]() {
        constexpr int NM = [] { int n = 0; for (int t = 0; t < 8; ++t) if ((TM >> t) & 1) n += t + 1; return n; }(), NR = 4 * (TMAX + 1);
        constexpr int PER = (NR + NM - 1) / NM;
#pragma unroll
        for (int i = 0; i < NM; ++i) { SGB(0x8, 1); SGB(0x100, PER); }
    };
    rd(0, 0);
#pragma unroll 1
    for (int nu = 0; nu < 8; nu += 2) {
        SCHED();
        rd(1, nu + 1); mm(0); sched();
        SCHED();
        rd(0, nu + 2 < 8 ? nu + 2 : 7); mm(1); sched();
    }
    SCHED();
}

template <int TM> DEVI void a_finish(LAS char* lds, int ch, int lane, float dskip, const f32x4 (&acc)[8]) {
    const int fi = lane & 15, gq = lane >> 4;
    LAS char* y = lds + ch * A_KSZ + ch * 16;
    LAS char* u0 = lds + A_U0 + ch * A_USZ;
#pragma unroll
    for (int t = 0; t < 8; ++t) {
        if (!((TM >> t) & 1)) continue;
#pragma unroll
        for (int hh = 0; hh < 2; ++hh) {
            const int tt = 256 * t + 32 * (fi >> 1) + 2 * (fi & 1) + 8 * gq + 4 * hh;
            const int e = tt + 224, R = e >> 5, un = ((e >> 2) & 7) ^ (((R >> 2) & 1) << 2);
            const unsigned uu = *(LAS unsigned*)(u0 + 64 * R + 8 * un + 2 * (e & 3));
            const float s0 = rbf(__uint_as_float(uu << 16) * dskip), s1 = rbf(__uint_as_float(uu & 0xffff0000u) * dskip);
            *(LAS unsigned*)(y + 2 * tt) = pk2(rbf(acc[t][2 * hh]) + s0, rbf(acc[t][2 * hh + 1]) + s1);
        }
    }
}

DEVI void a_block(const Params& p, LAS char* lds, int xl, int ja, int wave, int lane) {
    STAMP_DECL;
    STAMP(0); CSTAMP(14);
    const int tid = threadIdx.x, d0 = 72 * xl + 9 * ja;
    unsigned short uv[9][4];
    const __amdgpu_buffer_rsrc_t ru = mkrsrc(p.Au, L * D * 2);
    const float dsk = bf2f(p.ADskip[d0 + wave]), dsk8 = bf2f(p.ADskip[d0 + 8]);
    if (tid == 0) *(LAS unsigned*)(lds + A_FLAG) = 0;
    a_kgen(p, lds, wave, d0 + wave, lane);
    STAMP(1);
#pragma unroll
    for (int r = 0; r < 9; ++r) {
        const int id = r * 512 + tid, grp = ((id / 9) + 64 * ja) & 511, ch = id % 9;
#pragma unroll
        for (int i = 0; i < 4; ++i) uv[r][i] = __builtin_amdgcn_raw_buffer_load_b16(ru, (4 * grp + i) * (D * 2) + (d0 + ch) * 2, 0, 0);
    }
    for (int i = tid; i < 9 * 2 * 57; i += 512) {
        const int ch = i / 114, k = i % 114, cp = k / 57, un = k % 57;
        LAS char* ub = lds + A_U0 + ch * A_USZ + cp * A_UC;
        const int R = un >> 3, pu = (un & 7) ^ (((R >> 2) & 1) << 2) ^ cp;
        if (un < 56) *(LAS u32x2*)(ub + 64 * R + 8 * pu) = (u32x2){0u, 0u};
        else if (cp) *(LAS unsigned*)(ub + 64 * R + 8 * pu) = 0u;
    }
#pragma unroll
    for (int r = 0; r < 9; ++r) {
        const int id = r * 512 + tid, grp = ((id / 9) + 64 * ja) & 511, ch = id % 9;
        const unsigned lo = bfpair(uv[r][0], uv[r][1]), hi = bfpair(uv[r][2], uv[r][3]);
        LAS char* ub = lds + A_U0 + ch * A_USZ;
        const int un = grp + 56, R = un >> 3, sw = ((R >> 2) & 1) << 2;
        *(LAS u32x2*)(ub + 64 * R + 8 * ((un & 7) ^ sw)) = (u32x2){lo, hi};
        *(LAS unsigned*)(ub + A_UC + 64 * R + 8 * ((un & 7) ^ sw ^ 1) + 4) = lo;
        const int un1 = un + 1, R1 = un1 >> 3, sw1 = ((R1 >> 2) & 1) << 2;
        *(LAS unsigned*)(ub + A_UC + 64 * R1 + 8 * ((un1 & 7) ^ sw1 ^ 1)) = hi;
    }
    if (wave == 4) a_kgen(p, lds, 8, d0 + 8, lane);
    STAMP(2);
    BAR();
    STAMP(3);
    f32x4 acc[8];
#pragma unroll
    for (int t = 0; t < 8; ++t) acc[t] = (f32x4){0, 0, 0, 0};
    a_conv<0xff>(lds, wave, lane, acc);
    STAMP(4);
    a_finish<0xff>(lds, wave, lane, dsk, acc);
    STAMP(5);
    if (wave < 4) {
#pragma unroll
        for (int t = 0; t < 8; ++t) acc[t] = (f32x4){0, 0, 0, 0};
        if (wave == 0) { a_conv<0x81>(lds, 8, lane, acc); }
        else if (wave == 1) { a_conv<0x42>(lds, 8, lane, acc); }
        else if (wave == 2) { a_conv<0x24>(lds, 8, lane, acc); }
        else { a_conv<0x18>(lds, 8, lane, acc); }
    }
    STAMP(6);
    BAR();
    if (wave == 0) a_finish<0x81>(lds, 8, lane, dsk8, acc);
    else if (wave == 1) a_finish<0x42>(lds, 8, lane, dsk8, acc);
    else if (wave == 2) a_finish<0x24>(lds, 8, lane, dsk8, acc);
    else if (wave == 3) a_finish<0x18>(lds, 8, lane, dsk8, acc);
    BAR();
    STAMP(7);
    const __amdgpu_buffer_rsrc_t ro = mkrsrc(p.out, L * OUTW * 4);
#pragma unroll
    for (int r = 0; r < 12; ++r) {
        const int id = r * 512 + tid, row = id / 3, th = id % 3;
        typedef __attribute__((ext_vector_type(3))) unsigned u32x3;
        u32x3 v;
#pragma unroll
        for (int i = 0; i < 3; ++i) { const int ch = 3 * th + i; v[i] = (unsigned)*(LAS u16*)(lds + ch * A_KSZ + ch * 16 + 2 * row) << 16; }
        __builtin_amdgcn_raw_buffer_store_b96(v, ro, (row * OUTW + d0 + 3 * th) * 4, 0, 0);
    }
    STAMP(8); CSTAMP(15); STAMP(9);
    if (wave == 0) STAMP_OUT(0);
    if (wave == 4) STAMP_OUT(16);
}

DEVI void closer(const Params& p) {
    const int lane = threadIdx.x & 63;
    GAS unsigned* Ew = (GAS unsigned*)(p.ws + WS_E);
    const unsigned tag = next_tag(__builtin_amdgcn_readfirstlane(__hip_atomic_load(Ew, __ATOMIC_RELAXED, __HIP_MEMORY_SCOPE_AGENT)));
    GAS unsigned* fl = (GAS unsigned*)(p.ws + WS_FLAG);
    for (unsigned spins = 0; spins < SPIN_CAP; ++spins) {
        bool ok = true;
#pragma unroll
        for (int i = 0; i < 3; ++i) ok &= __hip_atomic_load(fl + (lane + 64 * i) * 32, __ATOMIC_RELAXED, __HIP_MEMORY_SCOPE_AGENT) == tag;
        if (__all(ok)) break;
        __builtin_amdgcn_s_sleep(8);
    }
    if (lane == 0) __hip_atomic_store(Ew, tag, __ATOMIC_RELAXED, __HIP_MEMORY_SCOPE_AGENT);
}

__global__ __launch_bounds__(512, 2) void fused_kernel(Params p) {
    extern __shared__ __attribute__((aligned(16))) char smem[];
    LAS char* lds = (LAS char*)smem;
    const int b = blockIdx.x, xl = b & 7, j = b >> 3;
    asm volatile("" :: "s"(p.Bx), "s"(p.BBre), "s"(p.BBim), "s"(p.BCre), "s"(p.BCim), "s"(p.ws), "s"(p.out), "s"(p.BDskip), "s"(p.BLre), "s"(p.BLim), "s"(p.Blogdt));
    asm volatile("" :: "s"(p.Au), "s"(p.AlogA), "s"(p.Aim), "s"(p.ACre), "s"(p.ACim), "s"(p.Alogdt), "s"(p.ADskip));
    const int wave = __builtin_amdgcn_readfirstlane(threadIdx.x >> 6), lane = threadIdx.x & 63;
    if (j < 24) { if (wave >= 4) b_loader(p, lds, xl, j, wave, lane); else b_compute(p, lds, xl, j, wave, lane); }
    else { a_block(p, lds, xl, j - 24, wave, lane); if (b == 255 && wave == 7) closer(p); }
}

extern "C" void kernel_launch(void* const* d_in, const int* in_sizes, int n_in, void* d_out, int out_size, void* d_ws, size_t ws_size, hipStream_t stream) {
    Params p;
    const u16* const* in = (const u16* const*)d_in;
    p.Au = in[0]; p.AlogA = in[1]; p.Aim = in[2]; p.ACre = in[3]; p.ACim = in[4]; p.Alogdt = in[5]; p.ADskip = in[6];
    p.Bx = in[7]; p.BLre = in[8]; p.BLim = in[9]; p.BBre = in[10]; p.BBim = in[11]; p.BCre = in[12]; p.BCim = in[13]; p.Blogdt = in[14]; p.BDskip = in[15];
    p.out = (float*)d_out; p.ws = (char*)d_ws;
    (void)hipFuncSetAttribute((const void*)fused_kernel, hipFuncAttributeMaxDynamicSharedMemorySize, LDS_BYTES);
    fused_kernel<<<256, 512, LDS_BYTES, stream>>>(p);
}
```

```cpp
#include <hip/hip_runtime.h>
#include <stdint.h>
typedef unsigned short u16;
typedef __attribute__((ext_vector_type(8))) short bf16x8;
typedef __attribute__((ext_vector_type(4))) short s16x4;
typedef __attribute__((ext_vector_type(4))) float f32x4;
typedef __attribute__((ext_vector_type(2))) float f32x2;
typedef __attribute__((ext_vector_type(4))) unsigned u32x4;
typedef __attribute__((ext_vector_type(2))) unsigned u32x2;
#define LAS __attribute__((address_space(3)))
#define GAS __attribute__((address_space(1)))
#define DEVI __device__ __forceinline__
constexpr int L = 2048, D = 576, P = 576, OUTW = 1152;
constexpr size_t WS_E = 0, WS_FLAG = 4096, WS_SG = 65536, WS_HT = 1 << 20, WS_ST = 200u << 20;
#define OUT_AUX 16
constexpr unsigned SPIN_CAP = 1u << 20;

struct Params {
    const u16 *Au, *AlogA, *Aim, *ACre, *ACim, *Alogdt, *ADskip;
    const u16 *Bx, *BLre, *BLim, *BBre, *BBim, *BCre, *BCim, *Blogdt, *BDskip;
    float* out; char* ws;
};

DEVI float bf2f(u16 v) { return __uint_as_float(((unsigned)v) << 16); }
DEVI float rbf(float f) { return (float)(__bf16)f; }
DEVI unsigned pk2(float a, float b) {
    typedef __attribute__((ext_vector_type(2))) __bf16 bf2;
    bf2 v = {(__bf16)a, (__bf16)b};
    return __builtin_bit_cast(unsigned, v);
}
struct cf { float r, i; };
DEVI cf cmul(cf a, cf b) { return {a.r * b.r - a.i * b.i, a.r * b.i + a.i * b.r}; }
DEVI cf cfma(cf a, cf b, cf c) { return {fmaf(a.r, b.r, fmaf(-a.i, b.i, c.r)), fmaf(a.r, b.i, fmaf(a.i, b.r, c.i))}; }
DEVI void sincos_cw(float x, float& s, float& c) {
    float k = __builtin_rintf(x * 0.15915494309189535f);
    float r = fmaf(k, -6.28125f, x);
    r = fmaf(k, -1.9350051879882812e-3f, r);
    r = fmaf(k, -3.0199159819567528e-7f, r);
    float rev = r * 0.15915494309189535f;
    s = __builtin_amdgcn_sinf(rev); c = __builtin_amdgcn_cosf(rev);
}
#define BAR() asm volatile("s_waitcnt lgkmcnt(0)\n\ts_barrier" ::: "memory")
#define SBAR() do { __builtin_amdgcn_sched_barrier(0); asm volatile("s_barrier" ::: "memory"); __builtin_amdgcn_sched_barrier(0); } while (0)
#define SCHED() __builtin_amdgcn_sched_barrier(0)
#define SGB(mask, n) __builtin_amdgcn_sched_group_barrier(mask, n, 0)
#define PIN2(a) asm volatile("" : "+v"((a).r), "+v"((a).i))
#define WAITV(n) asm volatile("s_waitcnt vmcnt(%0)" :: "n"(n) : "memory")
#define STAMP(i)
#define CSTAMP(i)
#define STAMP_DECL
#define STAMP_OUT(s)

DEVI __amdgpu_buffer_rsrc_t mkrsrc(const void* ptr, unsigned bytes) { return __builtin_amdgcn_make_buffer_rsrc((void*)ptr, 0, bytes, 0x00020000); }
template <int AUX> DEVI void dma16(__amdgpu_buffer_rsrc_t r, LAS char* dst, unsigned voff, unsigned soff) {
    __builtin_amdgcn_raw_ptr_buffer_load_lds(r, (LAS void*)dst, 16, voff, soff, 0, AUX);
}
DEVI f32x4 mfma16(bf16x8 a, bf16x8 b, f32x4 c) { return __builtin_amdgcn_mfma_f32_16x16x32_bf16(a, b, c, 0, 0, 0); }
DEVI f32x2 fma2(f32x2 a, f32x2 b, f32x2 c) { return __builtin_elementwise_fma(a, b, c); }
DEVI f32x2 splat2(float a) { return (f32x2){a, a}; }
DEVI void rows4(float x, float& x0, float& x1, float& x2, float& x3) {
    const unsigned u = __float_as_uint(x);
    const auto a = __builtin_amdgcn_permlane16_swap(u, u, false, false);
    const auto b = __builtin_amdgcn_permlane32_swap(a[0], a[0], false, false);
    const auto c = __builtin_amdgcn_permlane32_swap(a[1], a[1], false, false);
    x0 = __uint_as_float(b[0]); x2 = __uint_as_float(b[1]); x1 = __uint_as_float(c[0]); x3 = __uint_as_float(c[1]);
}
DEVI float rowsum4(float x) {
    const unsigned u = __float_as_uint(x);
    const auto a = __builtin_amdgcn_permlane16_swap(u, u, false, false);
    const float s = __uint_as_float(a[0]) + __uint_as_float(a[1]);
    const unsigned v = __float_as_uint(s);
    const auto b = __builtin_amdgcn_permlane32_swap(v, v, false, false);
    return __uint_as_float(b[0]) + __uint_as_float(b[1]);
}
DEVI unsigned next_tag(unsigned e) { return e * 1664525u + 1013904223u; }

constexpr int G1_SLOT = 28672, G1_XB = 16384, G1_NS = 5;
constexpr int G2_CS = 12288, G2_NC = 10, G2_HS = 8192, G2_NH = 4, G2_H0 = G2_CS * G2_NC;
constexpr int G2_CP1 = 5;
constexpr int M_WD = 155648, M_XW = M_WD + 15 * 384, M_XP = M_XW + 4 * 384, M_TAG = M_XP + 2 * 384;
constexpr int LDS_BYTES = 163840;
static_assert(M_TAG + 16 <= LDS_BYTES, "lds");

DEVI cf csel(bool c, cf a, cf b) { cf r; r.r = c ? a.r : b.r; r.i = c ? a.i : b.i; return r; }

DEVI void b_loader(const Params& p, LAS char* lds, int xl, int j, int wave, int lane) {
    STAMP_DECL;
    STAMP(0);
    const int c = 4 * (xl >> 1) + j / 6, g = 6 * (xl & 1) + j % 6;
    const int hc = 4 * xl + j / 6, dg = j % 6;
    const int lw = wave - 4, r8 = lane >> 3, sl = lane & 7;
    {
        const __amdgpu_buffer_rsrc_t rx = mkrsrc(p.Bx, L * D * 2), rbr = mkrsrc(p.BBre, P * D * 2), rbi = mkrsrc(p.BBim, P * D * 2);
        unsigned vx[4], vb[3];
#pragma unroll
        for (int i = 0; i < 4; ++i) { const int r = 32 * lw + 8 * i + r8; vx[i] = (128 * c + r) * (D * 2) + ((sl ^ ((r >> 2) & 7)) << 4); }
#pragma unroll
        for (int i = 0; i < 3; ++i) { const int rho = 24 * lw + 8 * i + r8; const int pt = rho >> 5, pp = rho & 15;
            vb[i] = (48 * g + 16 * pt + pp) * (D * 2) + ((sl ^ ((rho >> 1) & 7)) << 4); }
        const int rot1 = ((j % 6) * 3 / 2 + 2 * (j / 6)) % 9;
        auto g1_issue = [&](int s) {
            LAS char* slot = lds + (s % G1_NS) * G1_SLOT;
            const int sg = s + rot1 >= 9 ? s + rot1 - 9 : s + rot1;
#pragma unroll
            for (int i = 0; i < 4; ++i) dma16<0>(rx, slot + (32 * lw + 8 * i) * 128, vx[i], sg * 128);
#pragma unroll
            for (int i = 0; i < 3; ++i) { const int rho0 = 24 * lw + 8 * i;
                if ((rho0 >> 4) & 1) dma16<0>(rbi, slot + G1_XB + rho0 * 128, vb[i], sg * 128);
                else dma16<0>(rbr, slot + G1_XB + rho0 * 128, vb[i], sg * 128); }
        };
#pragma unroll
        for (int s = 0; s < 5; ++s) g1_issue(s);
        STAMP(1);
        WAITV(28); BAR();
        STAMP(2);
        WAITV(21); BAR();
        WAITV(14); BAR(); g1_issue(5);
        WAITV(14); BAR(); g1_issue(6);
        WAITV(14); BAR(); g1_issue(7);
        WAITV(14); BAR(); g1_issue(8);
        WAITV(14); BAR();
        WAITV(7); BAR();
        WAITV(0); BAR();
    }
    STAMP(3);
    BAR();
    const unsigned tag = __builtin_amdgcn_readfirstlane(*(LAS unsigned*)(lds + M_TAG));
    const int rot2 = (3 * dg + (9 * (j / 6)) / 2) % 18;
    const unsigned vce = (96 * dg + r8) * (P * 2) + ((sl ^ (r8 >> 1)) << 4), vco = (96 * dg + r8) * (P * 2) + ((sl ^ (4 + (r8 >> 1))) << 4);
    auto c_piece = [&](int s, int rg) {
        const int sg = s + rot2 >= 18 ? s + rot2 - 18 : s + rot2;
        const __amdgpu_buffer_rsrc_t rc = mkrsrc(sg < 9 ? p.BCre : p.BCim, D * P * 2);
        dma16<0>(rc, lds + (s % G2_NC) * G2_CS + rg * 1024, (rg & 1) ? vco : vce, (sg < 9 ? sg : sg - 9) * 128 + rg * 8 * P * 2);
    };
    auto c_issue = [&](int s) {
#pragma unroll
        for (int i = 0; i < 3; ++i) c_piece(s, 3 * lw + i);
    };
    if (lw < 2) {
#pragma unroll
        for (int s = 0; s < G2_CP1; ++s)
#pragma unroll
            for (int i = 0; i < 6; ++i) c_piece(s, 6 * lw + i);
    } else {
        const int pl = lane & 15, q = lane >> 4;
        cf H[3];
#pragma unroll
        for (int pt = 0; pt < 3; ++pt) H[pt] = {0.f, 0.f};
        if (8 * (lw - 2) < c) {
            const __amdgpu_buffer_rsrc_t rs = mkrsrc(p.ws + WS_SG, 16 * 12 * 48 * 16);
            int cp[2]; unsigned off[2];
#pragma unroll
            for (int r = 0; r < 2; ++r) { cp[r] = c - 1 - (8 * (lw - 2) + 4 * r + q); off[r] = (((cp[r] < 0 ? 0 : cp[r]) * 12 + g) * 48 + pl) * 16; }
            u32x4 v[2][3];
            for (unsigned spins = 0; spins < SPIN_CAP; ++spins) {
                bool ok = true;
#pragma unroll
                for (int r = 0; r < 2; ++r)
#pragma unroll
                    for (int pt = 0; pt < 3; ++pt) v[r][pt] = __builtin_amdgcn_raw_buffer_load_b128(rs, off[r] + pt * 256, 0, 16);
#pragma unroll
                for (int r = 0; r < 2; ++r)
#pragma unroll
                    for (int pt = 0; pt < 3; ++pt) ok &= cp[r] < 0 || ((v[r][pt][1] == tag) & (v[r][pt][3] == tag));
                if (__all(ok)) break;
                asm volatile("" ::: "memory");
            }
#pragma unroll
            for (int r = 0; r < 2; ++r) {
                const int dl = 8 * (lw - 2) + 4 * r + q;
#pragma unroll
                for (int pt = 0; pt < 3; ++pt) {
                    const f32x2 wv = *(LAS f32x2*)(lds + M_WD + (dl < 15 ? dl : 14) * 384 + (pt * 16 + pl) * 8);
                    cf s = {__uint_as_float(v[r][pt][0]), __uint_as_float(v[r][pt][2])};
                    if (cp[r] < 0) s = {0.f, 0.f};
                    H[pt] = cfma((cf){wv.x, wv.y}, s, H[pt]);
                }
            }
#pragma unroll
            for (int pt = 0; pt < 3; ++pt) { H[pt].r = rowsum4(H[pt].r); H[pt].i = rowsum4(H[pt].i); }
        }
        if (q == 0) {
#pragma unroll
            for (int pt = 0; pt < 3; ++pt) *(LAS f32x2*)(lds + M_XP + (lw - 2) * 384 + (pt * 16 + pl) * 8) = (f32x2){H[pt].r, H[pt].i};
        }
    }
    STAMP(4);
    BAR();
    BAR();
    STAMP(5);
#pragma unroll
    for (int s = G2_CP1; s < G2_NC; ++s) c_issue(s);
    const __amdgpu_buffer_rsrc_t rh = mkrsrc(p.ws + WS_HT, 32 * 1152 * 128);
    unsigned vh[2];
#pragma unroll
    for (int i = 0; i < 2; ++i) { const int k = 16 * lw + 8 * i + r8; const int f = ((k >> 1) & 1) | (((k >> 3) & 1) << 1);
        vh[i] = (hc * 1152 + k) * 128 + ((sl ^ (f << 1)) << 4); }
    auto h_issue = [&](int s) {
        LAS char* slot = lds + G2_H0 + (s % G2_NH) * G2_HS;
        const int sg = s + rot2 >= 18 ? s + rot2 - 18 : s + rot2;
#pragma unroll
        for (int i = 0; i < 2; ++i) dma16<16>(rh, slot + (16 * lw + 8 * i) * 128, vh[i], sg * 64 * 128);
    };
    {
        GAS unsigned* fl = (GAS unsigned*)(p.ws + WS_FLAG) + ((hc >> 1) * 12 + (lane < 12 ? lane : 0)) * 32;
        for (unsigned spins = 0; spins < SPIN_CAP; ++spins) {
            const unsigned v = __hip_atomic_load(fl, __ATOMIC_RELAXED, __HIP_MEMORY_SCOPE_AGENT);
            if (__all(v == tag)) break;
        }
        asm volatile("s_waitcnt vmcnt(0)" ::: "memory");
    }
    STAMP(6);
#pragma unroll
    for (int s = 0; s < G2_NH; ++s) h_issue(s);
#define G2_AFTER(need, ngroups) [] { int n = 0; bool seen = false, seenc = (need) < G2_NC; \
            for (int q = 0; q < G2_NH; ++q) { if (seen) n += 2; if (q == (need)) seen = true; } \
            for (int t = 0; t < (ngroups); ++t) { \
                if (t + G2_NH < 18) { if (seen && seenc) n += 2; if (t + G2_NH == (need)) seen = true; } \
                if (t + G2_NC < 18) { if (seen && seenc) n += 3; if (t + G2_NC == (need)) seenc = true; } } \
            return n; }()
#define G2_STEP(s) do { constexpr int after_ = G2_AFTER((s) + 1, (s) - 1 > 0 ? (s) - 1 : 0); WAITV(after_); BAR(); \
        if ((s) >= 1) { if ((s) - 1 + G2_NH < 18) h_issue((s) - 1 + G2_NH); if ((s) - 1 + G2_NC < 18) c_issue((s) - 1 + G2_NC); } } while (0)
    { constexpr int after_ = G2_AFTER(0, 0); WAITV(after_); BAR(); }
    STAMP(7);
    G2_STEP(0); G2_STEP(1); G2_STEP(2); G2_STEP(3); G2_STEP(4); G2_STEP(5); G2_STEP(6); G2_STEP(7); G2_STEP(8);
    G2_STEP(9); G2_STEP(10); G2_STEP(11); G2_STEP(12); G2_STEP(13); G2_STEP(14); G2_STEP(15); G2_STEP(16);
    STAMP(8);
    if (wave == 4) STAMP_OUT(16);
    if (wave == 6) STAMP_OUT(32);
}

DEVI void b_compute(const Params& p, LAS char* lds, int xl, int j, int w, int lane) {
    STAMP_DECL;
    STAMP(0); CSTAMP(14);
    const int c = 4 * (xl >> 1) + j / 6, g = 6 * (xl & 1) + j % 6;
    const int hc = 4 * xl + j / 6, dg = j % 6;
    const int pl = lane & 15, q = lane >> 4;
    const unsigned eraw = __hip_atomic_load((GAS unsigned*)(p.ws + WS_E), __ATOMIC_RELAXED, __HIP_MEMORY_SCOPE_AGENT);
    cf lam[3], lam4[3], lam8[3], lam32[3], l8q[3], w8q[3], aq[3], cf2[3];
#pragma unroll
    for (int pt = 0; pt < 3; ++pt) {
        const int pp = 48 * g + 16 * pt + pl;
        const float dt = rbf(expf(bf2f(p.Blogdt[pp]))), lr = bf2f(p.BLre[pp]), li = bf2f(p.BLim[pp]);
        float sn, cs; sincos_cw(dt * li, sn, cs);
        const float e = expf(dt * lr);
        lam[pt] = {e * cs, e * sn};
        const float br = lam[pt].r - 1.f, bi = lam[pt].i, den = 1.f / (lr * lr + li * li);
        cf2[pt] = {2.f * (br * lr + bi * li) * den, 2.f * (bi * lr - br * li) * den};
        const cf l2 = cmul(lam[pt], lam[pt]);
        lam4[pt] = cmul(l2, l2); lam8[pt] = cmul(lam4[pt], lam4[pt]);
        const cf l16 = cmul(lam8[pt], lam8[pt]); lam32[pt] = cmul(l16, l16);
        const cf one = {1.f, 0.f};
        cf t = csel(q & 1, lam8[pt], one); l8q[pt] = csel(q & 2, cmul(t, l16), t);
        t = csel(q & 1, one, lam8[pt]); w8q[pt] = csel(q & 2, t, cmul(t, l16));
        const cf l64 = cmul(lam32[pt], lam32[pt]);
        t = (w & 1) ? lam32[pt] : one; t = (w & 2) ? cmul(t, l64) : t;
        aq[pt] = cmul(t, l8q[pt]);
        const cf W1 = cmul(l64, l64), W2 = cmul(W1, W1), W4 = cmul(W2, W2), W8 = cmul(W4, W4);
        t = csel(q & 1, W1, one); t = csel(q & 2, cmul(t, W2), t); t = (w & 1) ? cmul(t, W4) : t; t = (w & 2) ? cmul(t, W8) : t;
        if (4 * w + q < 15) *(LAS f32x2*)(lds + M_WD + (4 * w + q) * 384 + (pt * 16 + pl) * 8) = (f32x2){t.r, t.i};
    }
    const unsigned tag = next_tag(__builtin_amdgcn_readfirstlane(eraw));
    if (w == 0 && lane == 0) *(LAS unsigned*)(lds + M_TAG) = tag;
#pragma unroll
    for (int pt = 0; pt < 3; ++pt) { PIN2(lam[pt]); PIN2(lam4[pt]); PIN2(lam8[pt]); PIN2(lam32[pt]); PIN2(l8q[pt]); PIN2(w8q[pt]); PIN2(aq[pt]); PIN2(cf2[pt]); }
    f32x4 acc[2][3][2];
#pragma unroll
    for (int rt = 0; rt < 2; ++rt)
#pragma unroll
        for (int pt = 0; pt < 3; ++pt) { acc[rt][pt][0] = (f32x4){0, 0, 0, 0}; acc[rt][pt][1] = (f32x4){0, 0, 0, 0}; }
    unsigned aoff[2][2], boff[2];
#pragma unroll
    for (int rt = 0; rt < 2; ++rt) { const int r = 32 * w + 8 * (pl >> 2) + 4 * (pl & 1) + 2 * rt + ((pl >> 1) & 1);
#pragma unroll
        for (int kk = 0; kk < 2; ++kk) aoff[rt][kk] = r * 128 + (((4 * kk + q) ^ ((r >> 2) & 7)) << 4); }
#pragma unroll
    for (int kk = 0; kk < 2; ++kk) boff[kk] = G1_XB + pl * 128 + (((4 * kk + q) ^ ((pl >> 1) & 7)) << 4);
    STAMP(1);
    bf16x8 fa[2][2], fb[2][6];
    auto g1_read = [&](int sl, int kk) {
        LAS char* slot = lds + sl * G1_SLOT;
#pragma unroll
        for (int rt = 0; rt < 2; ++rt) fa[kk][rt] = *(LAS bf16x8*)(slot + aoff[rt][kk]);
#pragma unroll
        for (int t = 0; t < 6; ++t) fb[kk][t] = *(LAS bf16x8*)(slot + boff[kk] + t * 2048);
    };
    auto g1_mma = [&](int kk) {
#pragma unroll
        for (int rt = 0; rt < 2; ++rt)
#pragma unroll
            for (int t = 0; t < 6; ++t) acc[rt][t >> 1][t & 1] = mfma16(fa[kk][rt], fb[kk][t], acc[rt][t >> 1][t & 1]);
    };
    auto g1_sched = [&]() {
#pragma unroll
        for (int i = 0; i < 8; ++i) { SGB(0x8, 1); SGB(0x100, 1); }
        SGB(0x8, 4);
    };
    BAR();
    STAMP(2);
    g1_read(0, 0);
    int sl0 = 0;
#pragma unroll 1
    for (int s = 0; s < 8; ++s) {
        SBAR();
        const int sl1 = sl0 == G1_NS - 1 ? 0 : sl0 + 1;
        g1_read(sl0, 1);
        g1_mma(0);
        g1_sched();
        SCHED();
        g1_read(sl1, 0);
        g1_mma(1);
        g1_sched();
        sl0 = sl1;
    }
    SCHED();
    g1_read(sl0, 1);
    g1_mma(0);
    g1_sched();
    SCHED();
    g1_mma(1);
    SCHED();
    STAMP(3);
    f32x2 Lr[3][4], Li[3][4];
#pragma unroll
    for (int pt = 0; pt < 3; ++pt) { Lr[pt][0] = (f32x2){acc[0][pt][0][0], acc[0][pt][0][1]}; Li[pt][0] = (f32x2){acc[0][pt][1][0], acc[0][pt][1][1]}; }
#pragma unroll
    for (int st_ = 1; st_ < 4; ++st_) {
        f32x2 tr[3], ti[3];
#pragma unroll
        for (int pt = 0; pt < 3; ++pt) {
            const f32x2 br = {acc[st_ >> 1][pt][0][2 * (st_ & 1)], acc[st_ >> 1][pt][0][2 * (st_ & 1) + 1]};
            const f32x2 bi = {acc[st_ >> 1][pt][1][2 * (st_ & 1)], acc[st_ >> 1][pt][1][2 * (st_ & 1) + 1]};
            tr[pt] = fma2(splat2(-lam[pt].i), Li[pt][st_ - 1], br); ti[pt] = fma2(splat2(lam[pt].i), Lr[pt][st_ - 1], bi);
        }
#pragma unroll
        for (int pt = 0; pt < 3; ++pt) { Lr[pt][st_] = fma2(splat2(lam[pt].r), Lr[pt][st_ - 1], tr[pt]); Li[pt][st_] = fma2(splat2(lam[pt].r), Li[pt][st_ - 1], ti[pt]); }
    }
    cf e[3];
    {
        cf t[3];
#pragma unroll
        for (int pt = 0; pt < 3; ++pt) e[pt] = cfma(lam4[pt], (cf){Lr[pt][3].x, Li[pt][3].x}, (cf){Lr[pt][3].y, Li[pt][3].y});
#pragma unroll
        for (int pt = 0; pt < 3; ++pt) t[pt] = cmul(w8q[pt], e[pt]);
#pragma unroll
        for (int pt = 0; pt < 3; ++pt) { t[pt].r = rowsum4(t[pt].r); t[pt].i = rowsum4(t[pt].i); }
        if (q == 0) {
#pragma unroll
            for (int pt = 0; pt < 3; ++pt) *(LAS f32x2*)(lds + M_XW + w * 384 + (pt * 16 + pl) * 8) = (f32x2){t[pt].r, t[pt].i};
        }
    }
    BAR();
    if (w < 3 && c < 15) {
        const float l0r = lam32[0].r, l0i = lam32[0].i, l1r = lam32[1].r, l1i = lam32[1].i, l2r = lam32[2].r, l2i = lam32[2].i;
        cf l32; l32.r = w == 0 ? l0r : (w == 1 ? l1r : l2r); l32.i = w == 0 ? l0i : (w == 1 ? l1i : l2i);
        cf a = {0.f, 0.f};
#pragma unroll
        for (int ww = 0; ww < 4; ++ww) { const f32x2 f = *(LAS f32x2*)(lds + M_XW + ww * 384 + (w * 16 + pl) * 8); a = ww == 0 ? (cf){f.x, f.y} : cfma(l32, a, (cf){f.x, f.y}); }
        if (q == 0) {
            const __amdgpu_buffer_rsrc_t rs = mkrsrc(p.ws + WS_SG, 16 * 12 * 48 * 16);
            const u32x4 v = {__float_as_uint(a.r), tag, __float_as_uint(a.i), tag};
            __builtin_amdgcn_raw_buffer_store_b128(v, rs, ((c * 12 + g) * 48 + w * 16 + pl) * 16, 0, 16);
        }
    }
    SCHED();
    STAMP(4);
    const int dh = w & 1, lh = w >> 1;
    u32x2 xs[2][3]; u32x2 dsk[3];
#pragma unroll
    for (int dt_ = 0; dt_ < 3; ++dt_) {
        const int d0 = 96 * dg + 48 * dh + 16 * dt_ + 4 * q;
        dsk[dt_] = *(const GAS u32x2*)(p.BDskip + d0);
#pragma unroll
        for (int lt = 0; lt < 2; ++lt) xs[lt][dt_] = *(const GAS u32x2*)(p.Bx + (64 * hc + 32 * lh + 16 * lt + pl) * D + d0);
    }
    f32x2 A2r[3], A2i[3], B2r[3], B2i[3], Or_[3][4], Oi_[3][4];
    cf K[3][4];
#pragma unroll
    for (int pt = 0; pt < 3; ++pt) {
        cf e0, e1, e2, e3;
        rows4(e[pt].r, e0.r, e1.r, e2.r, e3.r); rows4(e[pt].i, e0.i, e1.i, e2.i, e3.i);
        const cf p2 = cfma(lam8[pt], e0, e1), p3 = cfma(lam8[pt], p2, e2);
        cf X; X.r = q == 0 ? 0.f : q == 1 ? e0.r : q == 2 ? p2.r : p3.r; X.i = q == 0 ? 0.f : q == 1 ? e0.i : q == 2 ? p2.i : p3.i;
        const f32x2 f0 = *(LAS f32x2*)(lds + M_XW + 0 * 384 + (pt * 16 + pl) * 8), f1 = *(LAS f32x2*)(lds + M_XW + 1 * 384 + (pt * 16 + pl) * 8), f2 = *(LAS f32x2*)(lds + M_XW + 2 * 384 + (pt * 16 + pl) * 8);
        const cf a1 = {f0.x, f0.y}, a2 = cfma(lam32[pt], a1, (cf){f1.x, f1.y}), a3 = cfma(lam32[pt], a2, (cf){f2.x, f2.y});
        cf Xw; Xw.r = w == 0 ? 0.f : w == 1 ? a1.r : w == 2 ? a2.r : a3.r; Xw.i = w == 0 ? 0.f : w == 1 ? a1.i : w == 2 ? a2.i : a3.i;
        const cf b1 = cfma(l8q[pt], Xw, X);
        const cf b2 = cfma(lam4[pt], b1, (cf){Lr[pt][3].x, Li[pt][3].x});
        const cf a2q = cmul(lam4[pt], aq[pt]);
        A2r[pt] = (f32x2){aq[pt].r, a2q.r}; A2i[pt] = (f32x2){aq[pt].i, a2q.i};
        B2r[pt] = (f32x2){b1.r, b2.r}; B2i[pt] = (f32x2){b1.i, b2.i};
        K[pt][0] = cmul(cf2[pt], lam[pt]);
#pragma unroll
        for (int st_ = 1; st_ < 4; ++st_) K[pt][st_] = cmul(K[pt][st_ - 1], lam[pt]);
#pragma unroll
        for (int st_ = 0; st_ < 4; ++st_) {
            Or_[pt][st_] = fma2(splat2(cf2[pt].r), Lr[pt][st_], splat2(-cf2[pt].i) * Li[pt][st_]);
            Oi_[pt][st_] = fma2(splat2(-cf2[pt].r), Li[pt][st_], splat2(-cf2[pt].i) * Lr[pt][st_]);
        }
    }
#pragma unroll
    for (int pt = 0; pt < 3; ++pt) {
        asm volatile("" : "+v"(A2r[pt]), "+v"(A2i[pt]), "+v"(B2r[pt]), "+v"(B2i[pt]));
#pragma unroll
        for (int st_ = 0; st_ < 4; ++st_) { asm volatile("" : "+v"(Or_[pt][st_]), "+v"(Oi_[pt][st_])); PIN2(K[pt][st_]); }
    }
    STAMP(5);
    BAR();
    STAMP(10);
    {
        const __amdgpu_buffer_rsrc_t rh = mkrsrc(p.ws + WS_HT, 32 * 1152 * 128);
        const unsigned hoff = ((2 * c + (w >> 1)) * 1152 + 48 * g + pl) * 128 + (32 * (w & 1) + 8 * q) * 2;
        f32x2 cr[3], ci[3];
#pragma unroll
        for (int pt = 0; pt < 3; ++pt) {
            const f32x2 h0 = *(LAS f32x2*)(lds + M_XP + (pt * 16 + pl) * 8), h1 = *(LAS f32x2*)(lds + M_XP + 384 + (pt * 16 + pl) * 8);
            const f32x2 Hr = splat2(h0.x + h1.x), Hi = splat2(h0.y + h1.y);
            cr[pt] = fma2(A2r[pt], Hr, fma2(-A2i[pt], Hi, B2r[pt])); ci[pt] = fma2(A2r[pt], Hi, fma2(A2i[pt], Hr, B2i[pt]));
        }
#pragma unroll
        for (int pt = 0; pt < 3; ++pt) {
            f32x2 orr[4], oi[4];
#pragma unroll
            for (int st_ = 0; st_ < 4; ++st_) {
                orr[st_] = fma2(splat2(K[pt][st_].r), cr[pt], fma2(splat2(-K[pt][st_].i), ci[pt], Or_[pt][st_]));
                oi[st_] = fma2(splat2(-K[pt][st_].r), ci[pt], fma2(splat2(-K[pt][st_].i), cr[pt], Oi_[pt][st_]));
            }
            const u32x4 vr = {pk2(orr[0].x, orr[1].x), pk2(orr[2].x, orr[3].x), pk2(orr[0].y, orr[1].y), pk2(orr[2].y, orr[3].y)};
            const u32x4 vi = {pk2(oi[0].x, oi[1].x), pk2(oi[2].x, oi[3].x), pk2(oi[0].y, oi[1].y), pk2(oi[2].y, oi[3].y)};
            __builtin_amdgcn_raw_buffer_store_b128(vr, rh, hoff + pt * 16 * 128, 0, 16);
            __builtin_amdgcn_raw_buffer_store_b128(vi, rh, hoff + (576 + pt * 16) * 128, 0, 16);
        }
    }
    STAMP(11);
    f32x4 ac2[3][2];
#pragma unroll
    for (int a = 0; a < 3; ++a)
#pragma unroll
        for (int lt = 0; lt < 2; ++lt) {
            const unsigned x0 = xs[lt][a][0], x1 = xs[lt][a][1], d0 = dsk[a][0], d1 = dsk[a][1];
            ac2[a][lt][0] = rbf(__uint_as_float(x0 << 16) * __uint_as_float(d0 << 16));
            ac2[a][lt][1] = rbf(__uint_as_float(x0 & 0xffff0000u) * __uint_as_float(d0 & 0xffff0000u));
            ac2[a][lt][2] = rbf(__uint_as_float(x1 << 16) * __uint_as_float(d1 << 16));
            ac2[a][lt][3] = rbf(__uint_as_float(x1 & 0xffff0000u) * __uint_as_float(d1 & 0xffff0000u));
            asm volatile("" : "+v"(ac2[a][lt]));
        }
    WAITV(0);
    BAR();
    if (w == 0 && lane == 0) __hip_atomic_store((GAS unsigned*)(p.ws + WS_FLAG) + (c * 12 + g) * 32, tag, __ATOMIC_RELAXED, __HIP_MEMORY_SCOPE_AGENT);
    STAMP(6);
    unsigned coff[2], hoffs[2][2];
#pragma unroll
    for (int kk = 0; kk < 2; ++kk) coff[kk] = (48 * dh + pl) * 128 + (((4 * kk + q) ^ ((pl >> 1) & 7)) << 4);
    {
        const int a = pl >> 2, b = pl & 3;
#pragma unroll
        for (int hh = 0; hh < 2; ++hh) {
            const int k = 8 * q + 4 * hh + a;
            const int f = ((k >> 1) & 1) | (((k >> 3) & 1) << 1);
#pragma unroll
            for (int lt = 0; lt < 2; ++lt) { const int ch = 2 * (2 * lh + lt) + (b >> 1);
                hoffs[lt][hh] = G2_H0 + k * 128 + ((ch ^ (f << 1)) << 4) + (b & 1) * 8; }
        }
    }
    bf16x8 fc[2][2][3], fh[2][2][2];
    auto g2_read = [&](int bf, int cslot, int hslot) {
        LAS char* cs = lds + cslot * G2_CS; LAS char* hs = lds + hslot * G2_HS;
#pragma unroll
        for (int kk = 0; kk < 2; ++kk) {
#pragma unroll
            for (int lt = 0; lt < 2; ++lt) {
                const s16x4 lo = __builtin_amdgcn_ds_read_tr16_b64_v4i16((LAS s16x4*)(hs + kk * 4096 + hoffs[lt][0]));
                const s16x4 hi = __builtin_amdgcn_ds_read_tr16_b64_v4i16((LAS s16x4*)(hs + kk * 4096 + hoffs[lt][1]));
                fh[bf][kk][lt] = __builtin_shufflevector(lo, hi, 0, 1, 2, 3, 4, 5, 6, 7);
            }
#pragma unroll
            for (int a = 0; a < 3; ++a) fc[bf][kk][a] = *(LAS bf16x8*)(cs + coff[kk] + a * 2048);
        }
    };
    auto g2_mma = [&](int bf) {
#pragma unroll
        for (int kk = 0; kk < 2; ++kk)
#pragma unroll
            for (int a = 0; a < 3; ++a)
#pragma unroll
                for (int lt = 0; lt < 2; ++lt) ac2[a][lt] = mfma16(fc[bf][kk][a], fh[bf][kk][lt], ac2[a][lt]);
    };
    auto g2_sched = [&]() {
        SGB(0x8, 1); SGB(0x100, 2); SGB(0x8, 1); SGB(0x100, 2);
#pragma unroll
        for (int i = 0; i < 10; ++i) { SGB(0x8, 1); SGB(0x100, 1); }
    };
    SBAR();
    STAMP(7);
    g2_read(0, 0, 0);
    int cs1 = 1, hs1 = 1;
#pragma unroll 1
    for (int s = 0; s < 16; s += 2) {
        SBAR();
        g2_read(1, cs1, hs1); g2_mma(0); g2_sched();
        cs1 = cs1 == G2_NC - 1 ? 0 : cs1 + 1; hs1 = hs1 == G2_NH - 1 ? 0 : hs1 + 1;
        SBAR();
        g2_read(0, cs1, hs1); g2_mma(1); g2_sched();
        cs1 = cs1 == G2_NC - 1 ? 0 : cs1 + 1; hs1 = hs1 == G2_NH - 1 ? 0 : hs1 + 1;
    }
    SBAR();
    g2_read(1, cs1, hs1); g2_mma(0); g2_sched();
    SCHED();
    g2_mma(1);
    SCHED();
    STAMP(8);
#pragma unroll
    for (int a = 0; a < 3; ++a)
#pragma unroll
        for (int lt = 0; lt < 2; ++lt)
            __builtin_amdgcn_raw_buffer_store_b128(__builtin_bit_cast(u32x4, ac2[a][lt]), mkrsrc(p.out, L * OUTW * 4), ((64 * hc + 32 * lh + 16 * lt + pl) * OUTW + D + 96 * dg + 48 * dh + 16 * a + 4 * q) * 4, 0, OUT_AUX);
    STAMP(9); CSTAMP(15);
    if (w == 0) STAMP_OUT(0);
}

constexpr int A_KSZ = 8704, A_KB = 4224, A_U0 = 9 * A_KSZ, A_USZ = 9232, A_UC = 4608, A_FLAG = A_U0 + 9 * A_USZ;
constexpr int A_ROW = 544;
static_assert(A_FLAG + 16 <= LDS_BYTES, "lds");
struct c2 { f32x2 r, i; };
DEVI c2 c2mul(c2 a, c2 b) { c2 o; o.r = fma2(a.r, b.r, -(a.i * b.i)); o.i = fma2(a.r, b.i, a.i * b.r); return o; }
DEVI c2 c2exp(f32x2 are, f32x2 arev, float pw) {
    c2 o;
#pragma unroll
    for (int h = 0; h < 2; ++h) {
        const float e = __builtin_amdgcn_exp2f(are[h] * (pw * 1.4426950408889634f));
        const float f = __builtin_amdgcn_fractf(arev[h] * pw);
        o.r[h] = e * __builtin_amdgcn_cosf(f); o.i[h] = e * __builtin_amdgcn_sinf(f);
    }
    return o;
}
template <int ROW, bool NEG> DEVI void a_putrow(unsigned m0base, c2 v) {
    const unsigned d0 = pk2(v.r.x, NEG ? -v.i.x : v.i.x), d1 = pk2(v.r.y, NEG ? -v.i.y : v.i.y);
    asm volatile("s_mov_b32 m0, %0\n\ts_nop 0\n\tds_write_addtid_b32 %1 offset:%3\n\tds_write_addtid_b32 %2 offset:%4"
                 :: "s"(m0base), "v"(d0), "v"(d1), "n"(ROW * A_ROW), "n"(ROW * A_ROW + 256) : "memory");
}
DEVI unsigned bfpair(unsigned lo16, unsigned hi16) { return __builtin_amdgcn_perm(hi16, lo16, 0x05040100u); }

DEVI void a_kgen(const Params& p, LAS char* lds, int ch, int d, int lane) {
    LAS char* kreg = lds + ch * A_KSZ;
    const unsigned m0base = __builtin_amdgcn_readfirstlane(ch * A_KSZ);
    const float dt = rbf(expf(bf2f(p.Alogdt[d])));
    f32x2 are, arev; c2 V;
#pragma unroll
    for (int h = 0; h < 2; ++h) {
        const int n = d * 128 + lane + 64 * h;
        are[h] = -rbf(expf(bf2f(p.AlogA[n]))) * dt;
        arev[h] = (bf2f(p.Aim[n]) * dt) * 0.15915494309189535f;
        V.r[h] = 2.f * (bf2f(p.ACre[n]) * dt); V.i[h] = 2.f * (bf2f(p.ACim[n]) * dt);
    }
    const c2 w1 = c2exp(are, arev, 1.f), W = c2exp(are, arev, 128.f);
    const c2 w2 = c2mul(w1, w1), w3 = c2mul(w2, w1), w4 = c2mul(w2, w2), w5 = c2mul(w4, w1), w6 = c2mul(w4, w2), w7 = c2mul(w4, w3), w8 = c2mul(w4, w4);
    const int fi = lane & 15, gq = lane >> 4;
    LAS char* frag = kreg + fi * A_ROW + gq * 16;
    bf16x8 fb[8];
    f32x4 t0 = {0, 0, 0, 0}, t1 = {0, 0, 0, 0}, t2 = {0, 0, 0, 0};
    {
        c2 y; y.r = (f32x2){1.f, 1.f}; y.i = (f32x2){0.f, 0.f};
        a_putrow<0, true>(m0base, y); y = w8;
#define LO(j) a_putrow<j, true>(m0base, y); y = c2mul(y, w8);
        LO(1) LO(2) LO(3) LO(4) LO(5) LO(6) LO(7) LO(8) LO(9) LO(10) LO(11) LO(12) LO(13) LO(14)
#undef LO
        a_putrow<15, true>(m0base, y);
#pragma unroll
        for (int ks = 0; ks < 8; ++ks) fb[ks] = *(LAS bf16x8*)(frag + ks * 64);
        asm volatile("" ::: "memory");
    }
    {
        a_putrow<0, false>(m0base, V); a_putrow<1, false>(m0base, c2mul(V, w1)); a_putrow<2, false>(m0base, c2mul(V, w2)); a_putrow<3, false>(m0base, c2mul(V, w3));
        a_putrow<4, false>(m0base, c2mul(V, w4)); a_putrow<5, false>(m0base, c2mul(V, w5)); a_putrow<6, false>(m0base, c2mul(V, w6)); a_putrow<7, false>(m0base, c2mul(V, w7));
        V = c2mul(V, W);
        a_putrow<8, false>(m0base, V); a_putrow<9, false>(m0base, c2mul(V, w1)); a_putrow<10, false>(m0base, c2mul(V, w2)); a_putrow<11, false>(m0base, c2mul(V, w3));
        a_putrow<12, false>(m0base, c2mul(V, w4)); a_putrow<13, false>(m0base, c2mul(V, w5)); a_putrow<14, false>(m0base, c2mul(V, w6)); a_putrow<15, false>(m0base, c2mul(V, w7));
#pragma unroll
        for (int ks = 0; ks < 8; ++ks) t0 = mfma16(*(LAS bf16x8*)(frag + ks * 64), fb[ks], t0);
        asm volatile("" ::: "memory");
    }
    {
        V = c2mul(V, W);
        a_putrow<0, false>(m0base, V); a_putrow<1, false>(m0base, c2mul(V, w2)); a_putrow<2, false>(m0base, c2mul(V, w4)); a_putrow<3, false>(m0base, c2mul(V, w6));
        V = c2mul(V, W);
        a_putrow<4, false>(m0base, V); a_putrow<5, false>(m0base, c2mul(V, w2)); a_putrow<6, false>(m0base, c2mul(V, w4)); a_putrow<7, false>(m0base, c2mul(V, w6));
        V = c2mul(V, W); a_putrow<8, false>(m0base, V); a_putrow<9, false>(m0base, c2mul(V, w4));
        V = c2mul(V, W); a_putrow<10, false>(m0base, V); a_putrow<11, false>(m0base, c2mul(V, w4));
        V = c2mul(V, W); a_putrow<12, false>(m0base, V); a_putrow<13, false>(m0base, c2mul(V, w4));
        V = c2mul(V, W); a_putrow<14, false>(m0base, V); a_putrow<15, false>(m0base, c2mul(V, w4));
#pragma unroll
        for (int ks = 0; ks < 8; ++ks) t1 = mfma16(*(LAS bf16x8*)(frag + ks * 64), fb[ks], t1);
        asm volatile("" ::: "memory");
    }
    {
        V = c2mul(V, W); a_putrow<0, false>(m0base, V); V = c2mul(V, W); a_putrow<1, false>(m0base, V);
        V = c2mul(V, W); a_putrow<2, false>(m0base, V); V = c2mul(V, W); a_putrow<3, false>(m0base, V);
        V = c2mul(V, W); a_putrow<4, false>(m0base, V); V = c2mul(V, W); a_putrow<5, false>(m0base, V);
        V = c2mul(V, W); a_putrow<6, false>(m0base, V); V = c2mul(V, W); a_putrow<7, false>(m0base, V);
        V = c2mul(V, W); a_putrow<8, false>(m0base, V);
#pragma unroll
        for (int ks = 0; ks < 8; ++ks) t2 = mfma16(*(LAS bf16x8*)(frag + ks * 64), fb[ks], t2);
        asm volatile("" ::: "memory");
    }
    LAS char* tc = kreg;
    *(LAS u32x2*)(tc + 2 * (128 * (gq >> 1) + 4 * (gq & 1) + 8 * fi)) = (u32x2){pk2(t0[0], t0[1]), pk2(t0[2], t0[3])};
    if (gq < 2) *(LAS u32x2*)(tc + 2 * (256 + 64 * gq + 4 * fi)) = (u32x2){pk2(t1[0], t1[1]), pk2(t1[2], t1[3])};
    else { *(LAS unsigned*)(tc + 2 * (384 + 64 * (gq - 2) + 2 * fi)) = pk2(t1[0], t1[1]); *(LAS unsigned*)(tc + 2 * (384 + 64 * (gq - 2) + 32 + 2 * fi)) = pk2(t1[2], t1[3]); }
    if (gq < 2) {
#pragma unroll
        for (int r = 0; r < 4; ++r) *(LAS u16*)(tc + 2 * (512 + 16 * (4 * gq + r) + fi)) = (u16)pk2(t2[r], 0.f);
    } else if (lane == 32) *(LAS u16*)(tc + 2 * 640) = (u16)pk2(t2[0], 0.f);
    asm volatile("" ::: "memory");
    const unsigned eo3 = *(LAS unsigned*)(tc + 2 * (512 + 2 * lane)), n3 = *(LAS u16*)(tc + 2 * (512 + 2 * lane + 2));
    const unsigned eo2 = *(LAS unsigned*)(tc + 2 * (384 + 2 * lane)), n2 = *(LAS u16*)(tc + 2 * (384 + 2 * lane + 2));
    const unsigned eo1 = *(LAS unsigned*)(tc + 2 * (256 + 2 * lane)), n1 = *(LAS u16*)(tc + 2 * (256 + 2 * lane + 2));
    const u32x2 q0 = *(LAS u32x2*)(tc + 8 * lane);
    unsigned m0v = *(LAS u16*)(tc + 8 * lane - (lane ? 2 : 0));
    const unsigned t255 = *(LAS u16*)(tc + 2 * 255);
    asm volatile("" ::: "memory");
    if (lane == 0) m0v = 0;
    LAS char* ka = kreg; LAS char* kb = kreg + A_KB;
    {
        const unsigned E = eo3 & 0xffffu, O = eo3 >> 16, N = n3;
        const unsigned EE = bfpair(E, E), OO = bfpair(O, O), NN = bfpair(N, N);
        const int o = 2 * (2032 - 16 * (64 + lane));
        *(LAS u32x4*)(kb + o) = (u32x4){NN, NN, OO, OO}; *(LAS u32x4*)(kb + o + 16) = (u32x4){OO, bfpair(O, E), EE, EE};
        *(LAS u32x4*)(ka + o) = (u32x4){NN, bfpair(N, O), OO, OO}; *(LAS u32x4*)(ka + o + 16) = (u32x4){OO, EE, EE, EE};
    }
    {
        const unsigned E = eo2 & 0xffffu, O = eo2 >> 16, N = n2;
        const unsigned EE = bfpair(E, E), OO = bfpair(O, O);
        const int o = 2 * (2040 - 8 * (64 + lane));
        *(LAS u32x4*)(kb + o) = (u32x4){bfpair(N, N), OO, bfpair(O, E), EE};
        *(LAS u32x4*)(ka + o) = (u32x4){bfpair(N, O), OO, EE, EE};
    }
    {
        const unsigned E = eo1 & 0xffffu, O = eo1 >> 16, N = n1, Em = lane == 0 ? t255 : E;
        const int o = 2 * (2044 - 4 * (64 + lane));
        *(LAS u32x2*)(kb + o) = (u32x2){bfpair(N, O), bfpair(E, E)};
        *(LAS u32x2*)(ka + o) = (u32x2){bfpair(O, E), bfpair(E, Em)};
    }
    {
        const unsigned T0 = q0[0] & 0xffffu, T1 = q0[0] >> 16, T2 = q0[1] & 0xffffu, T3 = q0[1] >> 16;
        const int o = 2 * (2044 - 4 * lane);
        *(LAS u32x2*)(kb + o) = (u32x2){bfpair(T3, T2), bfpair(T1, T0)};
        *(LAS u32x2*)(ka + o) = (u32x2){bfpair(T2, T1), bfpair(T0, m0v)};
    }
    if (lane < 7) { *(LAS u32x2*)(kb + 4096 + 8 * lane) = (u32x2){0u, 0u}; *(LAS u32x2*)(ka + 4096 + 8 * lane) = (u32x2){0u, 0u}; }
}

template <int TM> DEVI void a_conv(LAS char* lds, int ch, int lane, f32x4 (&acc)[8]) {
    constexpr int TMAX = TM & 128 ? 7 : TM & 64 ? 6 : TM & 32 ? 5 : 4;
    const int fi = lane & 15, gq = lane >> 4, par = fi & 1, hp = fi >> 1;
    LAS char* kp = lds + ch * A_KSZ + par * A_KB + 2 * (2044 - 4 * hp + 8 * gq) - 512 * 7;
    LAS char* ub = lds + A_U0 + ch * A_USZ + (par ? 0 : A_UC);
    bf16x8 fa[2][8], fu[2][8];
    auto rd = [&](int bf, int nu) {
        LAS char* k = kp - 64 * nu;
        const int R = hp - nu + 7;
        const unsigned un = (2 * gq) ^ (((R >> 2) & 1) << 2) ^ (par ? 0 : 1);
        LAS char* u0 = ub + 64 * R + 8 * un; LAS char* u1 = ub + 64 * R + 8 * (un ^ 1);
#pragma unroll
        for (int m = 0; m <= TMAX; ++m) {
            const u32x2 alo = *(volatile LAS u32x2*)(k + 512 * (7 - m)), ahi = *(volatile LAS u32x2*)(k + 512 * (7 - m) + 8);
            fa[bf][m] = __builtin_bit_cast(bf16x8, (u32x4){alo[0], alo[1], ahi[0], ahi[1]});
            bool need = false;
#pragma unroll
            for (int t = m; t < 8; ++t) need |= ((TM >> t) & 1) != 0;
            const u32x2 blo = *(volatile LAS u32x2*)(u0 + 512 * m), bhi = *(volatile LAS u32x2*)(u1 + 512 * m);
            fu[bf][m] = __builtin_bit_cast(bf16x8, (u32x4){blo[0], blo[1], bhi[0], bhi[1]});
        }
    };
    auto mm = [&](int bf) {
#pragma unroll
        for (int dg_ = 0; dg_ <= 2 * TMAX; ++dg_)
#pragma unroll
            for (int m = 0; m <= TMAX; ++m) {
                const int kp_ = dg_ - m;
                if (kp_ < 0 || kp_ > TMAX || m + kp_ > 7) continue;
                if (m > kp_ && false) continue;
                const int t = m + kp_;
                if (t == dg_ && ((TM >> t) & 1)) acc[t] = mfma16(fa[bf][m], fu[bf][kp_], acc[t]);
            }
    };
    auto sched = [&]() {
        constexpr int NM = [] { int n = 0; for (int t = 0; t < 8; ++t) if ((TM >> t) & 1) n += t + 1; return n; }(), NR = 4 * (TMAX + 1);
        constexpr int PER = (NR + NM - 1) / NM;
#pragma unroll
        for (int i = 0; i < NM; ++i) { SGB(0x8, 1); SGB(0x100, PER); }
    };
    rd(0, 0);
#pragma unroll 1
    for (int nu = 0; nu < 8; nu += 2) {
        SCHED();
        rd(1, nu + 1); mm(0); sched();
        SCHED();
        rd(0, nu + 2 < 8 ? nu + 2 : 7); mm(1); sched();
    }
    SCHED();
}

template <int TM> DEVI void a_finish(LAS char* lds, int ch, int lane, float dskip, const f32x4 (&acc)[8]) {
    const int fi = lane & 15, gq = lane >> 4;
    LAS char* y = lds + ch * A_KSZ + ch * 16;
    LAS char* u0 = lds + A_U0 + ch * A_USZ;
#pragma unroll
    for (int t = 0; t < 8; ++t) {
        if (!((TM >> t) & 1)) continue;
#pragma unroll
        for (int hh = 0; hh < 2; ++hh) {
            const int tt = 256 * t + 32 * (fi >> 1) + 2 * (fi & 1) + 8 * gq + 4 * hh;
            const int e = tt + 224, R = e >> 5, un = ((e >> 2) & 7) ^ (((R >> 2) & 1) << 2);
            const unsigned uu = *(LAS unsigned*)(u0 + 64 * R + 8 * un + 2 * (e & 3));
            const float s0 = rbf(__uint_as_float(uu << 16) * dskip), s1 = rbf(__uint_as_float(uu & 0xffff0000u) * dskip);
            *(LAS unsigned*)(y + 2 * tt) = pk2(rbf(acc[t][2 * hh]) + s0, rbf(acc[t][2 * hh + 1]) + s1);
        }
    }
}

DEVI void a_block(const Params& p, LAS char* lds, int xl, int ja, int wave, int lane) {
    STAMP_DECL;
    STAMP(0); CSTAMP(14);
    const int tid = threadIdx.x, d0 = 72 * xl + 9 * ja;
    unsigned short uv[9][4];
    const __amdgpu_buffer_rsrc_t ru = mkrsrc(p.Au, L * D * 2);
    const float dsk = bf2f(p.ADskip[d0 + wave]), dsk8 = bf2f(p.ADskip[d0 + 8]);
    if (tid == 0) *(LAS unsigned*)(lds + A_FLAG) = 0;
    a_kgen(p, lds, wave, d0 + wave, lane);
    STAMP(1);
#pragma unroll
    for (int r = 0; r < 9; ++r) {
        const int id = r * 512 + tid, grp = ((id / 9) + 64 * ja) & 511, ch = id % 9;
#pragma unroll
        for (int i = 0; i < 4; ++i) uv[r][i] = __builtin_amdgcn_raw_buffer_load_b16(ru, (4 * grp + i) * (D * 2) + (d0 + ch) * 2, 0, 0);
    }
    for (int i = tid; i < 9 * 2 * 57; i += 512) {
        const int ch = i / 114, k = i % 114, cp = k / 57, un = k % 57;
        LAS char* ub = lds + A_U0 + ch * A_USZ + cp * A_UC;
        const int R = un >> 3, pu = (un & 7) ^ (((R >> 2) & 1) << 2) ^ cp;
        if (un < 56) *(LAS u32x2*)(ub + 64 * R + 8 * pu) = (u32x2){0u, 0u};
        else if (cp) *(LAS unsigned*)(ub + 64 * R + 8 * pu) = 0u;
    }
#pragma unroll
    for (int r = 0; r < 9; ++r) {
        const int id = r * 512 + tid, grp = ((id / 9) + 64 * ja) & 511, ch = id % 9;
        const unsigned lo = bfpair(uv[r][0], uv[r][1]), hi = bfpair(uv[r][2], uv[r][3]);
        LAS char* ub = lds + A_U0 + ch * A_USZ;
        const int un = grp + 56, R = un >> 3, sw = ((R >> 2) & 1) << 2;
        *(LAS u32x2*)(ub + 64 * R + 8 * ((un & 7) ^ sw)) = (u32x2){lo, hi};
        *(LAS unsigned*)(ub + A_UC + 64 * R + 8 * ((un & 7) ^ sw ^ 1) + 4) = lo;
        const int un1 = un + 1, R1 = un1 >> 3, sw1 = ((R1 >> 2) & 1) << 2;
        *(LAS unsigned*)(ub + A_UC + 64 * R1 + 8 * ((un1 & 7) ^ sw1 ^ 1)) = hi;
    }
    if (wave == 4) a_kgen(p, lds, 8, d0 + 8, lane);
    STAMP(2);
    BAR();
    STAMP(3);
    f32x4 acc[8];
#pragma unroll
    for (int t = 0; t < 8; ++t) acc[t] = (f32x4){0, 0, 0, 0};
    a_conv<0xff>(lds, wave, lane, acc);
    STAMP(4);
    a_finish<0xff>(lds, wave, lane, dsk, acc);
    STAMP(5);
    if (wave < 4) {
#pragma unroll
        for (int t = 0; t < 8; ++t) acc[t] = (f32x4){0, 0, 0, 0};
        if (wave == 0) { a_conv<0x81>(lds, 8, lane, acc); }
        else if (wave == 1) { a_conv<0x42>(lds, 8, lane, acc); }
        else if (wave == 2) { a_conv<0x24>(lds, 8, lane, acc); }
        else { a_conv<0x18>(lds, 8, lane, acc); }
    }
    STAMP(6);
    BAR();
    if (wave == 0) a_finish<0x81>(lds, 8, lane, dsk8, acc);
    else if (wave == 1) a_finish<0x42>(lds, 8, lane, dsk8, acc);
    else if (wave == 2) a_finish<0x24>(lds, 8, lane, dsk8, acc);
    else if (wave == 3) a_finish<0x18>(lds, 8, lane, dsk8, acc);
    BAR();
    STAMP(7);
    const __amdgpu_buffer_rsrc_t ro = mkrsrc(p.out, L * OUTW * 4);
#pragma unroll
    for (int r = 0; r < 12; ++r) {
        const int id = r * 512 + tid, row = id / 3, th = id % 3;
        typedef __attribute__((ext_vector_type(3))) unsigned u32x3;
        u32x3 v;
#pragma unroll
        for (int i = 0; i < 3; ++i) { const int ch = 3 * th + i; v[i] = (unsigned)*(LAS u16*)(lds + ch * A_KSZ + ch * 16 + 2 * row) << 16; }
        __builtin_amdgcn_raw_buffer_store_b96(v, ro, (row * OUTW + d0 + 3 * th) * 4, 0, 0);
    }
    STAMP(8); CSTAMP(15); STAMP(9);
    if (wave == 0) STAMP_OUT(0);
    if (wave == 4) STAMP_OUT(16);
}

DEVI void closer(const Params& p) {
    const int lane = threadIdx.x & 63;
    GAS unsigned* Ew = (GAS unsigned*)(p.ws + WS_E);
    const unsigned tag = next_tag(__builtin_amdgcn_readfirstlane(__hip_atomic_load(Ew, __ATOMIC_RELAXED, __HIP_MEMORY_SCOPE_AGENT)));
    GAS unsigned* fl = (GAS unsigned*)(p.ws + WS_FLAG);
    for (unsigned spins = 0; spins < SPIN_CAP; ++spins) {
        bool ok = true;
#pragma unroll
        for (int i = 0; i < 3; ++i) ok &= __hip_atomic_load(fl + (lane + 64 * i) * 32, __ATOMIC_RELAXED, __HIP_MEMORY_SCOPE_AGENT) == tag;
        if (__all(ok)) break;
        __builtin_amdgcn_s_sleep(8);
    }
    if (lane == 0) __hip_atomic_store(Ew, tag, __ATOMIC_RELAXED, __HIP_MEMORY_SCOPE_AGENT);
}

__global__ __launch_bounds__(512, 2) void fused_kernel(Params p) {
    unsigned long long tstart_;
    asm volatile("s_memrealtime %0\n\ts_waitcnt lgkmcnt(0)" : "=s"(tstart_) :: "memory");
    extern __shared__ __attribute__((aligned(16))) char smem[];
    LAS char* lds = (LAS char*)smem;
    const int b = blockIdx.x, xl = b & 7, j = b >> 3;
    asm volatile("" :: "s"(p.Bx), "s"(p.BBre), "s"(p.BBim), "s"(p.BCre), "s"(p.BCim), "s"(p.ws), "s"(p.out), "s"(p.BDskip), "s"(p.BLre), "s"(p.BLim), "s"(p.Blogdt));
    asm volatile("" :: "s"(p.Au), "s"(p.AlogA), "s"(p.Aim), "s"(p.ACre), "s"(p.ACim), "s"(p.Alogdt), "s"(p.ADskip));
    const int wave = __builtin_amdgcn_readfirstlane(threadIdx.x >> 6), lane = threadIdx.x & 63;
    if (j < 24) { if (wave >= 4) b_loader(p, lds, xl, j, wave, lane); else b_compute(p, lds, xl, j, wave, lane); }
    else { a_block(p, lds, xl, j - 24, wave, lane); if (b == 255 && wave == 7) closer(p); }
    if (false) while (__builtin_amdgcn_s_memrealtime() - tstart_ < 0ull * 100ull) __builtin_amdgcn_s_sleep(4);
}

extern "C" void kernel_launch(void* const* d_in, const int* in_sizes, int n_in, void* d_out, int out_size, void* d_ws, size_t ws_size, hipStream_t stream) {
    Params p;
    const u16* const* in = (const u16* const*)d_in;
    p.Au = in[0]; p.AlogA = in[1]; p.Aim = in[2]; p.ACre = in[3]; p.ACim = in[4]; p.Alogdt = in[5]; p.ADskip = in[6];
    p.Bx = in[7]; p.BLre = in[8]; p.BLim = in[9]; p.BBre = in[10]; p.BBim = in[11]; p.BCre = in[12]; p.BCim = in[13]; p.Blogdt = in[14]; p.BDskip = in[15];
    p.out = (float*)d_out; p.ws = (char*)d_ws;
    (void)hipFuncSetAttribute((const void*)fused_kernel, hipFuncAttributeMaxDynamicSharedMemorySize, LDS_BYTES);
    fused_kernel<<<256, 512, LDS_BYTES, stream>>>(p);
}
```

```cpp
#include <hip/hip_runtime.h>
#include <stdint.h>
typedef unsigned short u16;
typedef __attribute__((ext_vector_type(8))) short bf16x8;
typedef __attribute__((ext_vector_type(4))) short s16x4;
typedef __attribute__((ext_vector_type(4))) float f32x4;
typedef __attribute__((ext_vector_type(2))) float f32x2;
typedef __attribute__((ext_vector_type(4))) unsigned u32x4;
typedef __attribute__((ext_vector_type(2))) unsigned u32x2;
#define LAS __attribute__((address_space(3)))
#define GAS __attribute__((address_space(1)))
#define DEVI __device__ __forceinline__
constexpr int L = 2048, D = 576, P = 576, OUTW = 1152;
constexpr size_t WS_E = 0, WS_FLAG = 4096, WS_AFLAG = 4096 + 192 * 128, WS_SG = 65536, WS_KT = 6u << 20, WS_HT = 1 << 20, WS_ST = 200u << 20;
constexpr int KT_STRIDE = 2688;
#define OUT_AUX 16
constexpr int CODE_SLICES = 12;
constexpr unsigned SPIN_CAP = 1u << 20;

struct Params {
    const u16 *Au, *AlogA, *Aim, *ACre, *ACim, *Alogdt, *ADskip;
    const u16 *Bx, *BLre, *BLim, *BBre, *BBim, *BCre, *BCim, *Blogdt, *BDskip;
    float* out; char* ws;
};

DEVI float bf2f(u16 v) { return __uint_as_float(((unsigned)v) << 16); }
DEVI float rbf(float f) { return (float)(__bf16)f; }
DEVI unsigned pk2(float a, float b) {
    typedef __attribute__((ext_vector_type(2))) __bf16 bf2;
    bf2 v = {(__bf16)a, (__bf16)b};
    return __builtin_bit_cast(unsigned, v);
}
struct cf { float r, i; };
DEVI cf cmul(cf a, cf b) { return {a.r * b.r - a.i * b.i, a.r * b.i + a.i * b.r}; }
DEVI cf cfma(cf a, cf b, cf c) { return {fmaf(a.r, b.r, fmaf(-a.i, b.i, c.r)), fmaf(a.r, b.i, fmaf(a.i, b.r, c.i))}; }
DEVI void sincos_cw(float x, float& s, float& c) {
    float k = __builtin_rintf(x * 0.15915494309189535f);
    float r = fmaf(k, -6.28125f, x);
    r = fmaf(k, -1.9350051879882812e-3f, r);
    r = fmaf(k, -3.0199159819567528e-7f, r);
    float rev = r * 0.15915494309189535f;
    s = __builtin_amdgcn_sinf(rev); c = __builtin_amdgcn_cosf(rev);
}
#define BAR() asm volatile("s_waitcnt lgkmcnt(0)\n\ts_barrier" ::: "memory")
#define SBAR() do { __builtin_amdgcn_sched_barrier(0); asm volatile("s_barrier" ::: "memory"); __builtin_amdgcn_sched_barrier(0); } while (0)
#define SCHED() __builtin_amdgcn_sched_barrier(0)
#define SGB(mask, n) __builtin_amdgcn_sched_group_barrier(mask, n, 0)
#define PIN2(a) asm volatile("" : "+v"((a).r), "+v"((a).i))
#define WAITV(n) asm volatile("s_waitcnt vmcnt(%0)" :: "n"(n) : "memory")
#define STAMP(i)
#define CSTAMP(i)
#define STAMP_DECL
#define STAMP_OUT(s)

DEVI __amdgpu_buffer_rsrc_t mkrsrc(const void* ptr, unsigned bytes) { return __builtin_amdgcn_make_buffer_rsrc((void*)ptr, 0, bytes, 0x00020000); }
template <int AUX> DEVI void dma16(__amdgpu_buffer_rsrc_t r, LAS char* dst, unsigned voff, unsigned soff) {
    __builtin_amdgcn_raw_ptr_buffer_load_lds(r, (LAS void*)dst, 16, voff, soff, 0, AUX);
}
DEVI f32x4 mfma16(bf16x8 a, bf16x8 b, f32x4 c) { return __builtin_amdgcn_mfma_f32_16x16x32_bf16(a, b, c, 0, 0, 0); }
DEVI f32x2 fma2(f32x2 a, f32x2 b, f32x2 c) { return __builtin_elementwise_fma(a, b, c); }
DEVI f32x2 splat2(float a) { return (f32x2){a, a}; }
DEVI void rows4(float x, float& x0, float& x1, float& x2, float& x3) {
    const unsigned u = __float_as_uint(x);
    const auto a = __builtin_amdgcn_permlane16_swap(u, u, false, false);
    const auto b = __builtin_amdgcn_permlane32_swap(a[0], a[0], false, false);
    const auto c = __builtin_amdgcn_permlane32_swap(a[1], a[1], false, false);
    x0 = __uint_as_float(b[0]); x2 = __uint_as_float(b[1]); x1 = __uint_as_float(c[0]); x3 = __uint_as_float(c[1]);
}
DEVI float rowsum4(float x) {
    const unsigned u = __float_as_uint(x);
    const auto a = __builtin_amdgcn_permlane16_swap(u, u, false, false);
    const float s = __uint_as_float(a[0]) + __uint_as_float(a[1]);
    const unsigned v = __float_as_uint(s);
    const auto b = __builtin_amdgcn_permlane32_swap(v, v, false, false);
    return __uint_as_float(b[0]) + __uint_as_float(b[1]);
}
DEVI cf cpow2(float l2a, float rev, float m) {
    const float e = __builtin_amdgcn_exp2f(l2a * m), f = __builtin_amdgcn_fractf(rev * m);
    return {e * __builtin_amdgcn_cosf(f), e * __builtin_amdgcn_sinf(f)};
}
DEVI void rows3(float x, float (&o)[3]) { float x3; rows4(x, o[0], o[1], o[2], x3); }
DEVI unsigned next_tag(unsigned e) { return e * 1664525u + 1013904223u; }

constexpr int G1_SLOT = 28672, G1_XB = 16384, G1_NS = 5;
constexpr int G2_CS = 12288, G2_NC = 10, G2_HS = 8192, G2_NH = 4, G2_H0 = G2_CS * G2_NC;
constexpr int G2_CP1 = 2, G2_CP2 = 6;
constexpr int B_KSCR = 0, G1_BASE = 2 * 8704;
constexpr int M_XW = 160768, M_XP = M_XW + 4 * 384, M_TAG = M_XP + 2 * 384, M_FULL = M_TAG + 16, M_FREE = M_TAG + 32;
constexpr int LDS_BYTES = 163840;
static_assert(M_FREE + 16 <= LDS_BYTES, "lds");

constexpr int A_KSZ = 8704, A_KB = 4224, A_U0 = 9 * A_KSZ, A_USZ = 9232, A_UC = 4608, A_FLAG = A_U0 + 9 * A_USZ, A_DUMMY = A_FLAG + 16;
constexpr int A_Y8 = 4352;
constexpr int A_ROW = 544;
static_assert(A_FLAG + 16 + 512 <= LDS_BYTES, "lds");
struct c2 { f32x2 r, i; };
DEVI c2 c2mul(c2 a, c2 b) { c2 o; o.r = fma2(a.r, b.r, -(a.i * b.i)); o.i = fma2(a.r, b.i, a.i * b.r); return o; }
DEVI c2 c2exp(f32x2 are, f32x2 arev, float pw) {
    c2 o;
#pragma unroll
    for (int h = 0; h < 2; ++h) {
        const float e = __builtin_amdgcn_exp2f(are[h] * (pw * 1.4426950408889634f));
        const float f = __builtin_amdgcn_fractf(arev[h] * pw);
        o.r[h] = e * __builtin_amdgcn_cosf(f); o.i[h] = e * __builtin_amdgcn_sinf(f);
    }
    return o;
}
template <bool NEG> DEVI void a_put(LAS char* row, c2 v) { *(LAS u32x2*)row = (u32x2){pk2(v.r.x, NEG ? -v.i.x : v.i.x), pk2(v.r.y, NEG ? -v.i.y : v.i.y)}; }
DEVI unsigned bfpair(unsigned lo16, unsigned hi16) { return __builtin_amdgcn_perm(hi16, lo16, 0x05040100u); }
struct AParam { unsigned logA, aim, cre, cim; u16 logdt; };
DEVI AParam a_params(const Params& p, int d, int lane) {
    AParam q;
    const int n = d * 64 + lane;
    q.logA = ((const GAS unsigned*)p.AlogA)[n]; q.aim = ((const GAS unsigned*)p.Aim)[n]; q.cre = ((const GAS unsigned*)p.ACre)[n]; q.cim = ((const GAS unsigned*)p.ACim)[n];
    q.logdt = p.Alogdt[d];
    return q;
}
DEVI float bfh(unsigned v, int h) { return __uint_as_float(h ? v & 0xffff0000u : v << 16); }

template <int ROW> DEVI void a_put4(unsigned m0base, u32x2 a, u32x2 b, u32x2 c, u32x2 d) {
    asm volatile("s_mov_b32 m0, %0\n\ts_nop 0\n\t"
                 "ds_write_addtid_b32 %1 offset:%9\n\tds_write_addtid_b32 %2 offset:%10\n\t"
                 "ds_write_addtid_b32 %3 offset:%11\n\tds_write_addtid_b32 %4 offset:%12\n\t"
                 "ds_write_addtid_b32 %5 offset:%13\n\tds_write_addtid_b32 %6 offset:%14\n\t"
                 "ds_write_addtid_b32 %7 offset:%15\n\tds_write_addtid_b32 %8 offset:%16"
                 :: "s"(m0base), "v"(a[0]), "v"(a[1]), "v"(b[0]), "v"(b[1]), "v"(c[0]), "v"(c[1]), "v"(d[0]), "v"(d[1]),
                    "n"(ROW * A_ROW), "n"(ROW * A_ROW + 256), "n"((ROW + 1) * A_ROW), "n"((ROW + 1) * A_ROW + 256),
                    "n"((ROW + 2) * A_ROW), "n"((ROW + 2) * A_ROW + 256), "n"((ROW + 3) * A_ROW), "n"((ROW + 3) * A_ROW + 256) : "memory");
}
DEVI void a_ktable(const AParam& q, LAS char* lds, int koff, int lane) {
    LAS char* kreg = lds + koff;
    const unsigned m0base = __builtin_amdgcn_readfirstlane(koff);
    const float dt = rbf(expf(bf2f(q.logdt)));
    f32x2 are, arev; c2 V;
#pragma unroll
    for (int h = 0; h < 2; ++h) {
        are[h] = -rbf(expf(bfh(q.logA, h))) * dt;
        arev[h] = (bfh(q.aim, h) * dt) * 0.15915494309189535f;
        V.r[h] = 2.f * (bfh(q.cre, h) * dt); V.i[h] = 2.f * (bfh(q.cim, h) * dt);
    }
    const c2 w1 = c2exp(are, arev, 1.f), W = c2exp(are, arev, 128.f);
    const c2 w2 = c2mul(w1, w1), w3 = c2mul(w2, w1), w4 = c2mul(w2, w2), w5 = c2mul(w4, w1), w6 = c2mul(w4, w2), w7 = c2mul(w4, w3), w8 = c2mul(w4, w4);
    const int fi = lane & 15, gq = lane >> 4;
    LAS char* frag = kreg + fi * A_ROW + gq * 16;
    bf16x8 fb[8], fa[8];
    u32x2 val[16];
    f32x4 t0 = {0, 0, 0, 0}, t1 = {0, 0, 0, 0}, t2 = {0, 0, 0, 0};
    auto pack = [&](c2 v, bool neg) { return (u32x2){pk2(v.r.x, neg ? -v.i.x : v.i.x), pk2(v.r.y, neg ? -v.i.y : v.i.y)}; };
    auto put = [&](int n) {
        a_put4<0>(m0base, val[0], val[1], val[2], val[3]); a_put4<4>(m0base, val[4], val[5], val[6], val[7]);
        if (n > 9) { a_put4<8>(m0base, val[8], val[9], val[10], val[11]); a_put4<12>(m0base, val[12], val[13], val[14], val[15]); }
        else a_put4<8>(m0base, val[8], val[8], val[8], val[8]);
    };
    auto get = [&](bf16x8 (&f)[8]) {
#pragma unroll
        for (int ks = 0; ks < 8; ++ks) f[ks] = *(LAS bf16x8*)(frag + ks * 64);
    };
    auto mm = [&](f32x4& t) {
#pragma unroll
        for (int ks = 0; ks < 8; ++ks) t = mfma16(fa[ks], fb[ks], t);
    };
    {
        c2 one; one.r = (f32x2){1.f, 1.f}; one.i = (f32x2){0.f, 0.f};
        const c2 w16 = c2mul(w8, w8), w24 = c2mul(w16, w8), w32 = c2mul(w16, w16);
        c2 a = one, b = w8, c = w16, d = w24;
        val[0] = pack(a, true); val[1] = pack(b, true); val[2] = pack(c, true); val[3] = pack(d, true);
        a = w32; b = c2mul(b, w32); c = c2mul(c, w32); d = c2mul(d, w32);
        val[4] = pack(a, true); val[5] = pack(b, true); val[6] = pack(c, true); val[7] = pack(d, true);
        a = c2mul(a, w32); b = c2mul(b, w32); c = c2mul(c, w32); d = c2mul(d, w32);
        val[8] = pack(a, true); val[9] = pack(b, true); val[10] = pack(c, true); val[11] = pack(d, true);
        a = c2mul(a, w32); b = c2mul(b, w32); c = c2mul(c, w32); d = c2mul(d, w32);
        val[12] = pack(a, true); val[13] = pack(b, true); val[14] = pack(c, true); val[15] = pack(d, true);
        put(16); get(fb);
        SCHED();
    }
    const c2 W2 = c2mul(W, W);
    c2 Ve = V, Vo = c2mul(V, W);
    {
        val[0] = pack(Ve, false); val[1] = pack(c2mul(Ve, w1), false); val[2] = pack(c2mul(Ve, w2), false); val[3] = pack(c2mul(Ve, w3), false);
        val[4] = pack(c2mul(Ve, w4), false); val[5] = pack(c2mul(Ve, w5), false); val[6] = pack(c2mul(Ve, w6), false); val[7] = pack(c2mul(Ve, w7), false);
        val[8] = pack(Vo, false); val[9] = pack(c2mul(Vo, w1), false); val[10] = pack(c2mul(Vo, w2), false); val[11] = pack(c2mul(Vo, w3), false);
        val[12] = pack(c2mul(Vo, w4), false); val[13] = pack(c2mul(Vo, w5), false); val[14] = pack(c2mul(Vo, w6), false); val[15] = pack(c2mul(Vo, w7), false);
        put(16); get(fa);
        SCHED();
    }
    {
        Ve = c2mul(Ve, W2); Vo = c2mul(Vo, W2);
        val[0] = pack(Ve, false); val[1] = pack(c2mul(Ve, w2), false); val[2] = pack(c2mul(Ve, w4), false); val[3] = pack(c2mul(Ve, w6), false);
        val[4] = pack(Vo, false); val[5] = pack(c2mul(Vo, w2), false); val[6] = pack(c2mul(Vo, w4), false); val[7] = pack(c2mul(Vo, w6), false);
        Ve = c2mul(Ve, W2); Vo = c2mul(Vo, W2);
        val[8] = pack(Ve, false); val[9] = pack(c2mul(Ve, w4), false); val[10] = pack(Vo, false); val[11] = pack(c2mul(Vo, w4), false);
        Ve = c2mul(Ve, W2); Vo = c2mul(Vo, W2);
        val[12] = pack(Ve, false); val[13] = pack(c2mul(Ve, w4), false); val[14] = pack(Vo, false); val[15] = pack(c2mul(Vo, w4), false);
        SCHED();
        mm(t0);
        SCHED();
        put(16); get(fa);
        SCHED();
    }
    {
        const c2 v8 = c2mul(Ve, W2), v9 = c2mul(Vo, W2), v10 = c2mul(v8, W2), v11 = c2mul(v9, W2);
        val[0] = pack(v8, false); val[1] = pack(v9, false); val[2] = pack(v10, false); val[3] = pack(v11, false);
        const c2 v12 = c2mul(v10, W2), v13 = c2mul(v11, W2), v14 = c2mul(v12, W2), v15 = c2mul(v13, W2), v16 = c2mul(v14, W2);
        val[4] = pack(v12, false); val[5] = pack(v13, false); val[6] = pack(v14, false); val[7] = pack(v15, false); val[8] = pack(v16, false);
        SCHED();
        mm(t1);
        SCHED();
        put(9); get(fa);
        SCHED();
        mm(t2);
        SCHED();
    }
    LAS char* tc = kreg;
    *(LAS u32x2*)(tc + 2 * (128 * (gq >> 1) + 4 * (gq & 1) + 8 * fi)) = (u32x2){pk2(t0[0], t0[1]), pk2(t0[2], t0[3])};
    {
        const int a0 = gq < 2 ? 256 + 64 * gq + 4 * fi : 384 + 64 * (gq - 2) + 2 * fi;
        *(LAS unsigned*)(tc + 2 * a0) = pk2(t1[0], t1[1]);
        *(LAS unsigned*)(tc + 2 * a0 + (gq < 2 ? 4 : 64)) = pk2(t1[2], t1[3]);
        const int b0 = gq < 2 ? 512 + 64 * gq + fi : (lane == 32 ? 640 : 704 + lane);
        const unsigned p01 = pk2(t2[0], t2[1]), p23 = pk2(t2[2], t2[3]);
        const int bs = gq < 2 ? 32 : 256;
        *(LAS u16*)(tc + 2 * b0) = (u16)p01; *(LAS u16*)(tc + 2 * b0 + bs) = (u16)(p01 >> 16);
        *(LAS u16*)(tc + 2 * b0 + 2 * bs) = (u16)p23; *(LAS u16*)(tc + 2 * b0 + 3 * bs) = (u16)(p23 >> 16);
    }
    asm volatile("" ::: "memory");
}

DEVI void a_expand(LAS char* kreg, int lane) {
    LAS char* tc = kreg;
    const unsigned eo3 = *(LAS unsigned*)(tc + 2 * (512 + 2 * lane)), n3 = *(LAS u16*)(tc + 2 * (512 + 2 * lane + 2));
    const unsigned eo2 = *(LAS unsigned*)(tc + 2 * (384 + 2 * lane)), n2 = *(LAS u16*)(tc + 2 * (384 + 2 * lane + 2));
    const unsigned eo1 = *(LAS unsigned*)(tc + 2 * (256 + 2 * lane)), n1 = *(LAS u16*)(tc + 2 * (256 + 2 * lane + 2));
    const u32x2 q0 = *(LAS u32x2*)(tc + 8 * lane);
    unsigned m0v = *(LAS u16*)(tc + 8 * lane - (lane ? 2 : 0));
    const unsigned t255 = *(LAS u16*)(tc + 2 * 255);
    asm volatile("" ::: "memory");
    if (lane == 0) m0v = 0;
    LAS char* ka = kreg; LAS char* kb = kreg + A_KB;
    {
        const unsigned E = eo3 & 0xffffu, O = eo3 >> 16, N = n3;
        const unsigned EE = bfpair(E, E), OO = bfpair(O, O), NN = bfpair(N, N);
        const int o = 2 * (2032 - 16 * (64 + lane));
        *(LAS u32x4*)(kb + o) = (u32x4){NN, NN, OO, OO}; *(LAS u32x4*)(kb + o + 16) = (u32x4){OO, bfpair(O, E), EE, EE};
        *(LAS u32x4*)(ka + o) = (u32x4){NN, bfpair(N, O), OO, OO}; *(LAS u32x4*)(ka + o + 16) = (u32x4){OO, EE, EE, EE};
    }
    {
        const unsigned E = eo2 & 0xffffu, O = eo2 >> 16, N = n2;
        const unsigned EE = bfpair(E, E), OO = bfpair(O, O);
        const int o = 2 * (2040 - 8 * (64 + lane));
        *(LAS u32x4*)(kb + o) = (u32x4){bfpair(N, N), OO, bfpair(O, E), EE};
        *(LAS u32x4*)(ka + o) = (u32x4){bfpair(N, O), OO, EE, EE};
    }
    {
        const unsigned E = eo1 & 0xffffu, O = eo1 >> 16, N = n1, Em = lane == 0 ? t255 : E;
        const int o = 2 * (2044 - 4 * (64 + lane));
        *(LAS u32x2*)(kb + o) = (u32x2){bfpair(N, O), bfpair(E, E)};
        *(LAS u32x2*)(ka + o) = (u32x2){bfpair(O, E), bfpair(E, Em)};
    }
    {
        const unsigned T0 = q0[0] & 0xffffu, T1 = q0[0] >> 16, T2 = q0[1] & 0xffffu, T3 = q0[1] >> 16;
        const int o = 2 * (2044 - 4 * lane);
        *(LAS u32x2*)(kb + o) = (u32x2){bfpair(T3, T2), bfpair(T1, T0)};
        *(LAS u32x2*)(ka + o) = (u32x2){bfpair(T2, T1), bfpair(T0, m0v)};
    }
    { const int z = lane < 7 ? lane : 6; *(LAS u32x2*)(kb + 4096 + 8 * z) = (u32x2){0u, 0u}; *(LAS u32x2*)(ka + 4096 + 8 * z) = (u32x2){0u, 0u}; }
}

DEVI cf csel(bool c, cf a, cf b) { cf r; r.r = c ? a.r : b.r; r.i = c ? a.i : b.i; return r; }

constexpr int g2_ops_after(int need, int ngroups) {
    int n = 0;
    for (int q = 0; q < G2_NH; ++q) { if (q == need) n = 0; else n += 2; }
    for (int q = G2_CP2; q < G2_NC; ++q) { if (q == need) n = 0; else n += 3; }
    for (int t = 0; t < ngroups; ++t) {
        if (t + G2_NH < 18) { if (t + G2_NH == need) n = 0; else n += 2; }
        if (t + G2_NC < 18) { if (t + G2_NC == need) n = 0; else n += 3; }
    }
    return n;
}
template <bool SLEEP = false> DEVI void lds_wait_ge(LAS char* lds, int off, int n, unsigned need) {
    for (unsigned spins = 0; spins < SPIN_CAP; ++spins) {
        unsigned m;
        if (n == 2) { const u32x2 f = *(volatile LAS u32x2*)(lds + off); m = min(f[0], f[1]); }
        else { const u32x4 f = *(volatile LAS u32x4*)(lds + off); m = min(min(f[0], f[1]), min(f[2], f[3])); }
        if (__builtin_amdgcn_readfirstlane(m) >= need) break;
        if (SLEEP) __builtin_amdgcn_s_sleep(1);
    }
    asm volatile("" ::: "memory");
}

DEVI void b_loader(const Params& p, LAS char* lds, int xl, int j, int wave, int lane, unsigned cpf) {
    STAMP_DECL;
    STAMP(0);
    const int c = 4 * (xl >> 1) + j / 6, g = 6 * (xl & 1) + j % 6;
    const int hc = 4 * xl + j / 6, dg = j % 6;
    const int lw = wave - 4, r8 = lane >> 3, sl = lane & 7;
    cf wd[2][3] = {};
    u16 bdt[3], blr[3], bli[3];
    if (lw < 2) {
        const __amdgpu_buffer_rsrc_t rx = mkrsrc(p.Bx, L * D * 2), rbr = mkrsrc(p.BBre, P * D * 2), rbi = mkrsrc(p.BBim, P * D * 2);
        unsigned vx[4], vb[2];
#pragma unroll
        for (int i = 0; i < 4; ++i) { const int r = 64 * lw + 8 * i + r8; vx[i] = (128 * c + r) * (D * 2) + ((sl ^ ((r >> 2) & 7)) << 4); }
#pragma unroll
        for (int i = 0; i < 2; ++i) vb[i] = (48 * g + r8) * (D * 2) + ((sl ^ (4 * i + (r8 >> 1))) << 4);
        const int rot1 = ((j % 6) * 3 / 2 + 2 * (j / 6)) % 9;
        auto g1_issue = [&](int s) {
            LAS char* slot = lds + G1_BASE + (s % G1_NS) * G1_SLOT;
            const int sg = s + rot1 >= 9 ? s + rot1 - 9 : s + rot1;
#pragma unroll
            for (int i = 0; i < 8; ++i) dma16<0>(rx, slot + (64 * lw + 8 * i) * 128, vx[i & 3], sg * 128 + (i >> 2) * 32 * D * 2);
#pragma unroll
            for (int i = 0; i < 6; ++i) {
                const int rho0 = 48 * lw + 8 * i, srow = 16 * (rho0 >> 5) + (rho0 & 15);
                const __amdgpu_buffer_rsrc_t rb = mkrsrc(((rho0 >> 4) & 1) ? p.BBim : p.BBre, P * D * 2);
                dma16<0>(rb, slot + G1_XB + rho0 * 128, vb[i & 1], sg * 128 + srow * D * 2);
            }
        };
        auto landed = [&](unsigned n) { if (lane == 0) *(volatile LAS unsigned*)(lds + M_FULL + 4 * lw) = n; };
        asm volatile("s_barrier" ::: "memory");
        g1_issue(0); g1_issue(1);
        STAMP(1);
        WAITV(14); landed(1); g1_issue(2);
        STAMP(2);
        WAITV(14); landed(2); g1_issue(3);
        WAITV(14); landed(3); g1_issue(4);
#pragma unroll 1
        for (int s = 5; s < 9; ++s) {
            WAITV(14); landed(s - 1);
            lds_wait_ge(lds, M_FREE, 4, s - 4);
            g1_issue(s);
        }
        WAITV(14); landed(8);
        WAITV(0); landed(9);
        asm volatile("" :: "v"(cpf));
    } else {
        const int slot = ((j * 8 + xl) * 2 + (lw - 2));
        const int ab = slot / 5, d = 72 * (ab & 7) + 9 * (ab >> 3) + 4 + slot % 5;
        AParam pq = {};
        const unsigned eraw = __hip_atomic_load((GAS unsigned*)(p.ws + WS_E), __ATOMIC_RELAXED, __HIP_MEMORY_SCOPE_AGENT);
        if (slot < 320) pq = a_params(p, d, lane);
#pragma unroll
        for (int pt = 0; pt < 3; ++pt) { const int pp = 48 * g + 16 * pt + (lane & 15); bdt[pt] = p.Blogdt[pp]; blr[pt] = p.BLre[pp]; bli[pt] = p.BLim[pp]; }
        asm volatile("s_barrier" ::: "memory");
        STAMP(1);
        if (slot < 320) {
            a_ktable(pq, lds, B_KSCR + (lw - 2) * A_KSZ, lane);
            const unsigned tg = next_tag(__builtin_amdgcn_readfirstlane(eraw));
            const __amdgpu_buffer_rsrc_t rk = mkrsrc(p.ws + WS_KT, 576 * KT_STRIDE);
            LAS char* tc = lds + B_KSCR + (lw - 2) * A_KSZ;
#pragma unroll
            for (int i = 0; i < 3; ++i) {
                const int pr = min(64 * i + lane, 160);
                const u32x2 v = *(LAS u32x2*)(tc + 8 * pr);
                __builtin_amdgcn_raw_buffer_store_b128((u32x4){v[0], tg, v[1], tg}, rk, d * KT_STRIDE + 16 * pr, 0, 16);
            }
        }
#pragma unroll
        for (int pt = 0; pt < 3; ++pt) {
            const float dt = rbf(expf(bf2f(bdt[pt]))), l2a = dt * bf2f(blr[pt]) * 1.4426950408889634f, rev = dt * bf2f(bli[pt]) * 0.15915494309189535f;
#pragma unroll
            for (int r = 0; r < 2; ++r) { wd[r][pt] = cpow2(l2a, rev, (float)(128 * (8 * (lw - 2) + 4 * r + (lane >> 4)))); PIN2(wd[r][pt]); }
        }
        asm volatile("" :: "v"(cpf), "v"(eraw));
        STAMP(2);
    }
    STAMP(3);
    BAR();
    BAR();
    const unsigned tag = __builtin_amdgcn_readfirstlane(*(LAS unsigned*)(lds + M_TAG));
    const int rot2 = (3 * dg + (9 * (j / 6)) / 2) % 18;
    const unsigned vce = (96 * dg + r8) * (P * 2) + ((sl ^ (r8 >> 1)) << 4), vco = (96 * dg + r8) * (P * 2) + ((sl ^ (4 + (r8 >> 1))) << 4);
    auto c_piece = [&](int s, int rg) {
        const int sg = s + rot2 >= 18 ? s + rot2 - 18 : s + rot2;
        const __amdgpu_buffer_rsrc_t rc = mkrsrc(sg < 9 ? p.BCre : p.BCim, D * P * 2);
        dma16<0>(rc, lds + (s % G2_NC) * G2_CS + rg * 1024, (rg & 1) ? vco : vce, (sg < 9 ? sg : sg - 9) * 128 + rg * 8 * P * 2);
    };
    auto c_issue = [&](int s) {
#pragma unroll
        for (int i = 0; i < 3; ++i) c_piece(s, 3 * lw + i);
    };
    if (lw < 2) {
#pragma unroll
        for (int s = 0; s < G2_CP1; ++s)
#pragma unroll
            for (int i = 0; i < 6; ++i) c_piece(s, 6 * lw + i);
    } else {
        const int pl = lane & 15, q = lane >> 4;
        cf H[3];
#pragma unroll
        for (int pt = 0; pt < 3; ++pt) H[pt] = {0.f, 0.f};
        if (8 * (lw - 2) < c) {
            const __amdgpu_buffer_rsrc_t rs = mkrsrc(p.ws + WS_SG, 16 * 12 * 48 * 16);
            int cp[2]; unsigned off[2];
#pragma unroll
            for (int r = 0; r < 2; ++r) { cp[r] = c - 1 - (8 * (lw - 2) + 4 * r + q); off[r] = (((cp[r] < 0 ? 0 : cp[r]) * 12 + g) * 48 + pl) * 16; }
            u32x4 v[2][3];
            for (unsigned spins = 0; spins < SPIN_CAP; ++spins) {
                bool ok = true;
#pragma unroll
                for (int r = 0; r < 2; ++r)
#pragma unroll
                    for (int pt = 0; pt < 3; ++pt) v[r][pt] = __builtin_amdgcn_raw_buffer_load_b128(rs, off[r] + pt * 256, 0, 16);
#pragma unroll
                for (int r = 0; r < 2; ++r)
#pragma unroll
                    for (int pt = 0; pt < 3; ++pt) ok &= cp[r] < 0 || ((v[r][pt][1] == tag) & (v[r][pt][3] == tag));
                if (__all(ok)) break;
                asm volatile("" ::: "memory");
            }
#pragma unroll
            for (int r = 0; r < 2; ++r) {
#pragma unroll
                for (int pt = 0; pt < 3; ++pt) {
                    cf s = {__uint_as_float(v[r][pt][0]), __uint_as_float(v[r][pt][2])};
                    if (cp[r] < 0) s = {0.f, 0.f};
                    H[pt] = cfma(wd[r][pt], s, H[pt]);
                }
            }
#pragma unroll
            for (int pt = 0; pt < 3; ++pt) { H[pt].r = rowsum4(H[pt].r); H[pt].i = rowsum4(H[pt].i); }
        }
        if (q == 0) {
#pragma unroll
            for (int pt = 0; pt < 3; ++pt) *(LAS f32x2*)(lds + M_XP + (lw - 2) * 384 + (pt * 16 + pl) * 8) = (f32x2){H[pt].r, H[pt].i};
        }
    }
    STAMP(4);
    BAR();
    BAR();
    STAMP(5);
#pragma unroll
    for (int s = G2_CP1; s < G2_CP2; ++s) c_issue(s);
    const __amdgpu_buffer_rsrc_t rh = mkrsrc(p.ws + WS_HT, 32 * 1152 * 128);
    unsigned vh[2];
#pragma unroll
    for (int i = 0; i < 2; ++i) { const int k = 16 * lw + 8 * i + r8; const int f = ((k >> 1) & 1) | (((k >> 3) & 1) << 1);
        vh[i] = (hc * 1152 + k) * 128 + ((sl ^ (f << 1)) << 4); }
    auto h_issue = [&](int s) {
        LAS char* slot = lds + G2_H0 + (s % G2_NH) * G2_HS;
        const int sg = s + rot2 >= 18 ? s + rot2 - 18 : s + rot2;
#pragma unroll
        for (int i = 0; i < 2; ++i) dma16<16>(rh, slot + (16 * lw + 8 * i) * 128, vh[i], sg * 64 * 128);
    };
    {
        GAS unsigned* fl = (GAS unsigned*)(p.ws + WS_FLAG) + ((hc >> 1) * 12 + (lane < 12 ? lane : 0)) * 32;
        for (unsigned spins = 0; spins < SPIN_CAP; ++spins) {
            const unsigned v = __hip_atomic_load(fl, __ATOMIC_RELAXED, __HIP_MEMORY_SCOPE_AGENT);
            if (__all(v == tag)) break;
        }
        asm volatile("s_waitcnt vmcnt(0)" ::: "memory");
    }
    STAMP(6);
#pragma unroll
    for (int s = 0; s < G2_NH; ++s) h_issue(s);
#pragma unroll
    for (int s = G2_CP2; s < G2_NC; ++s) c_issue(s);
#define G2_AFTER(need, ngroups) g2_ops_after(need, ngroups)
#define G2_STEP(s) do { constexpr int after_ = G2_AFTER((s) + 1, (s) - 1 > 0 ? (s) - 1 : 0); WAITV(after_); BAR(); \
        if ((s) >= 1) { if ((s) - 1 + G2_NH < 18) h_issue((s) - 1 + G2_NH); if ((s) - 1 + G2_NC < 18) c_issue((s) - 1 + G2_NC); } } while (0)
    { constexpr int after_ = G2_AFTER(0, 0); WAITV(after_); BAR(); }
    STAMP(7);
    G2_STEP(0); G2_STEP(1); G2_STEP(2); G2_STEP(3); G2_STEP(4); G2_STEP(5); G2_STEP(6); G2_STEP(7); G2_STEP(8);
    G2_STEP(9); G2_STEP(10); G2_STEP(11); G2_STEP(12); G2_STEP(13); G2_STEP(14); G2_STEP(15); G2_STEP(16);
    STAMP(8);
    if (wave == 4) STAMP_OUT(16);
    if (wave == 6) STAMP_OUT(32);
}

DEVI void b_compute(const Params& p, LAS char* lds, int xl, int j, int w, int lane, unsigned cpf) {
    STAMP_DECL;
    STAMP(0); CSTAMP(14);
    const int c = 4 * (xl >> 1) + j / 6, g = 6 * (xl & 1) + j % 6;
    const int hc = 4 * xl + j / 6, dg = j % 6;
    const int pl = lane & 15, q = lane >> 4;
    const unsigned eraw = __hip_atomic_load((GAS unsigned*)(p.ws + WS_E), __ATOMIC_RELAXED, __HIP_MEMORY_SCOPE_AGENT);
    const int ppq = 48 * g + 16 * (q < 3 ? q : 2) + pl;
    const u16 rdt = p.Blogdt[ppq], rlr = p.BLre[ppq], rli = p.BLim[ppq];
    if (w == 0 && lane < 8) *(LAS unsigned*)(lds + M_FULL + 4 * lane) = 0u;
    asm volatile("s_waitcnt lgkmcnt(0)\n\ts_barrier" ::: "memory");
    cf lam[3], lam4[3], lam32[3], w8q[3], cf2[3];
    float l2a[3], rev[3];
    {
        const float dt = rbf(expf(bf2f(rdt))), lr = bf2f(rlr), li = bf2f(rli);
        float sn, cs; sincos_cw(dt * li, sn, cs);
        const float e = expf(dt * lr), ar = e * cs, ai = e * sn;
        const float br = ar - 1.f, den = 2.f / (lr * lr + li * li);
        float t0[3], t1[3], t2[3], t3[3];
        rows3(ar, t0); rows3(ai, t1); rows3((br * lr + ai * li) * den, t2); rows3((ai * lr - br * li) * den, t3);
        rows3(dt * lr * 1.4426950408889634f, l2a); rows3(dt * li * 0.15915494309189535f, rev);
#pragma unroll
        for (int pt = 0; pt < 3; ++pt) { lam[pt] = {t0[pt], t1[pt]}; cf2[pt] = {t2[pt], t3[pt]}; }
    }
#pragma unroll
    for (int pt = 0; pt < 3; ++pt) {
        const cf l2 = cmul(lam[pt], lam[pt]);
        lam4[pt] = cmul(l2, l2);
        lam32[pt] = cpow2(l2a[pt], rev[pt], 32.f);
        w8q[pt] = cpow2(l2a[pt], rev[pt], (float)(24 - 8 * q));
    }
    asm volatile("" :: "v"(cpf));
    const unsigned tag = next_tag(__builtin_amdgcn_readfirstlane(eraw));
    if (w == 0 && lane == 0) *(LAS unsigned*)(lds + M_TAG) = tag;
#pragma unroll
    for (int pt = 0; pt < 3; ++pt) { PIN2(lam[pt]); PIN2(lam4[pt]); PIN2(lam32[pt]); PIN2(w8q[pt]); PIN2(cf2[pt]); asm volatile("" : "+v"(l2a[pt]), "+v"(rev[pt])); }
    f32x4 acc[2][3][2];
#pragma unroll
    for (int rt = 0; rt < 2; ++rt)
#pragma unroll
        for (int pt = 0; pt < 3; ++pt) { acc[rt][pt][0] = (f32x4){0, 0, 0, 0}; acc[rt][pt][1] = (f32x4){0, 0, 0, 0}; }
    unsigned aoff[2][2], boff[2];
#pragma unroll
    for (int rt = 0; rt < 2; ++rt) { const int r = 32 * w + 8 * (pl >> 2) + 4 * (pl & 1) + 2 * rt + ((pl >> 1) & 1);
#pragma unroll
        for (int kk = 0; kk < 2; ++kk) aoff[rt][kk] = r * 128 + (((4 * kk + q) ^ ((r >> 2) & 7)) << 4); }
#pragma unroll
    for (int kk = 0; kk < 2; ++kk) boff[kk] = G1_XB + pl * 128 + (((4 * kk + q) ^ ((pl >> 1) & 7)) << 4);
    STAMP(1);
    bf16x8 fa[2][2], fb[2][6];
    auto g1_read = [&](int sl, int kk) {
        LAS char* slot = lds + G1_BASE + sl * G1_SLOT;
#pragma unroll
        for (int rt = 0; rt < 2; ++rt) fa[kk][rt] = *(LAS bf16x8*)(slot + aoff[rt][kk]);
#pragma unroll
        for (int t = 0; t < 6; ++t) fb[kk][t] = *(LAS bf16x8*)(slot + boff[kk] + t * 2048);
    };
    auto g1_mma = [&](int kk) {
#pragma unroll
        for (int rt = 0; rt < 2; ++rt)
#pragma unroll
            for (int t = 0; t < 6; ++t) acc[rt][t >> 1][t & 1] = mfma16(fa[kk][rt], fb[kk][t], acc[rt][t >> 1][t & 1]);
    };
    auto g1_sched = [&]() {
#pragma unroll
        for (int i = 0; i < 8; ++i) { SGB(0x8, 1); SGB(0x100, 1); }
        SGB(0x8, 4);
    };
    lds_wait_ge(lds, M_FULL, 2, 1);
    STAMP(2);
    g1_read(0, 0);
    int sl0 = 0;
#pragma unroll 1
    for (int s = 0; s < 8; ++s) {
        SCHED();
        const int sl1 = sl0 == G1_NS - 1 ? 0 : sl0 + 1;
        const u32x2 ff = *(volatile LAS u32x2*)(lds + M_FULL);
        g1_read(sl0, 1);
        g1_mma(0);
        SGB(0x100, 1);
        g1_sched();
        SCHED();
        if (__builtin_amdgcn_readfirstlane(min(ff[0], ff[1])) < (unsigned)(s + 2)) lds_wait_ge(lds, M_FULL, 2, s + 2);
        SCHED();
        g1_read(sl1, 0);
        g1_mma(1);
        g1_sched();
        SCHED();
        if (lane == 0) *(volatile LAS unsigned*)(lds + M_FREE + 4 * w) = s + 1;
        sl0 = sl1;
    }
    SCHED();
    g1_read(sl0, 1);
    g1_mma(0);
    g1_sched();
    SCHED();
    g1_mma(1);
    SCHED();
    STAMP(3);
    f32x2 Lr[3][4], Li[3][4];
#pragma unroll
    for (int pt = 0; pt < 3; ++pt) { Lr[pt][0] = (f32x2){acc[0][pt][0][0], acc[0][pt][0][1]}; Li[pt][0] = (f32x2){acc[0][pt][1][0], acc[0][pt][1][1]}; }
#pragma unroll
    for (int st_ = 1; st_ < 4; ++st_) {
        f32x2 tr[3], ti[3];
#pragma unroll
        for (int pt = 0; pt < 3; ++pt) {
            const f32x2 br = {acc[st_ >> 1][pt][0][2 * (st_ & 1)], acc[st_ >> 1][pt][0][2 * (st_ & 1) + 1]};
            const f32x2 bi = {acc[st_ >> 1][pt][1][2 * (st_ & 1)], acc[st_ >> 1][pt][1][2 * (st_ & 1) + 1]};
            tr[pt] = fma2(splat2(-lam[pt].i), Li[pt][st_ - 1], br); ti[pt] = fma2(splat2(lam[pt].i), Lr[pt][st_ - 1], bi);
        }
#pragma unroll
        for (int pt = 0; pt < 3; ++pt) { Lr[pt][st_] = fma2(splat2(lam[pt].r), Lr[pt][st_ - 1], tr[pt]); Li[pt][st_] = fma2(splat2(lam[pt].r), Li[pt][st_ - 1], ti[pt]); }
    }
    cf e[3];
    {
        cf t[3];
#pragma unroll
        for (int pt = 0; pt < 3; ++pt) e[pt] = cfma(lam4[pt], (cf){Lr[pt][3].x, Li[pt][3].x}, (cf){Lr[pt][3].y, Li[pt][3].y});
#pragma unroll
        for (int pt = 0; pt < 3; ++pt) t[pt] = cmul(w8q[pt], e[pt]);
#pragma unroll
        for (int pt = 0; pt < 3; ++pt) { t[pt].r = rowsum4(t[pt].r); t[pt].i = rowsum4(t[pt].i); }
        if (q == 0) {
#pragma unroll
            for (int pt = 0; pt < 3; ++pt) *(LAS f32x2*)(lds + M_XW + w * 384 + (pt * 16 + pl) * 8) = (f32x2){t[pt].r, t[pt].i};
        }
    }
    BAR();
    if (w < 3 && c < 15) {
        const float l0r = lam32[0].r, l0i = lam32[0].i, l1r = lam32[1].r, l1i = lam32[1].i, l2r = lam32[2].r, l2i = lam32[2].i;
        cf l32; l32.r = w == 0 ? l0r : (w == 1 ? l1r : l2r); l32.i = w == 0 ? l0i : (w == 1 ? l1i : l2i);
        cf a = {0.f, 0.f};
#pragma unroll
        for (int ww = 0; ww < 4; ++ww) { const f32x2 f = *(LAS f32x2*)(lds + M_XW + ww * 384 + (w * 16 + pl) * 8); a = ww == 0 ? (cf){f.x, f.y} : cfma(l32, a, (cf){f.x, f.y}); }
        if (q == 0) {
            const __amdgpu_buffer_rsrc_t rs = mkrsrc(p.ws + WS_SG, 16 * 12 * 48 * 16);
            const u32x4 v = {__float_as_uint(a.r), tag, __float_as_uint(a.i), tag};
            __builtin_amdgcn_raw_buffer_store_b128(v, rs, ((c * 12 + g) * 48 + w * 16 + pl) * 16, 0, 16);
        }
    }
    asm volatile("s_barrier" ::: "memory");
    SCHED();
    STAMP(4);
    const int dh = w & 1, lh = w >> 1;
    u32x2 xs[2][3]; u32x2 dsk[3];
#pragma unroll
    for (int dt_ = 0; dt_ < 3; ++dt_) {
        const int d0 = 96 * dg + 48 * dh + 16 * dt_ + 4 * q;
        dsk[dt_] = *(const GAS u32x2*)(p.BDskip + d0);
#pragma unroll
        for (int lt = 0; lt < 2; ++lt) xs[lt][dt_] = *(const GAS u32x2*)(p.Bx + (64 * hc + 32 * lh + 16 * lt + pl) * D + d0);
    }
    f32x2 A2r[3], A2i[3], B2r[3], B2i[3], Or_[3][4], Oi_[3][4];
    cf K[3][4];
#pragma unroll
    for (int pt = 0; pt < 3; ++pt) {
        cf e0, e1, e2, e3;
        rows4(e[pt].r, e0.r, e1.r, e2.r, e3.r); rows4(e[pt].i, e0.i, e1.i, e2.i, e3.i);
        const cf lam8 = cmul(lam4[pt], lam4[pt]), l8q = cpow2(l2a[pt], rev[pt], (float)(8 * q)), aq = cpow2(l2a[pt], rev[pt], (float)(32 * w + 8 * q));
        const cf p2 = cfma(lam8, e0, e1), p3 = cfma(lam8, p2, e2);
        cf X; X.r = q == 0 ? 0.f : q == 1 ? e0.r : q == 2 ? p2.r : p3.r; X.i = q == 0 ? 0.f : q == 1 ? e0.i : q == 2 ? p2.i : p3.i;
        const f32x2 f0 = *(LAS f32x2*)(lds + M_XW + 0 * 384 + (pt * 16 + pl) * 8), f1 = *(LAS f32x2*)(lds + M_XW + 1 * 384 + (pt * 16 + pl) * 8), f2 = *(LAS f32x2*)(lds + M_XW + 2 * 384 + (pt * 16 + pl) * 8);
        const cf a1 = {f0.x, f0.y}, a2 = cfma(lam32[pt], a1, (cf){f1.x, f1.y}), a3 = cfma(lam32[pt], a2, (cf){f2.x, f2.y});
        cf Xw; Xw.r = w == 0 ? 0.f : w == 1 ? a1.r : w == 2 ? a2.r : a3.r; Xw.i = w == 0 ? 0.f : w == 1 ? a1.i : w == 2 ? a2.i : a3.i;
        const cf b1 = cfma(l8q, Xw, X);
        const cf b2 = cfma(lam4[pt], b1, (cf){Lr[pt][3].x, Li[pt][3].x});
        const cf a2q = cmul(lam4[pt], aq);
        A2r[pt] = (f32x2){aq.r, a2q.r}; A2i[pt] = (f32x2){aq.i, a2q.i};
        B2r[pt] = (f32x2){b1.r, b2.r}; B2i[pt] = (f32x2){b1.i, b2.i};
        K[pt][0] = cmul(cf2[pt], lam[pt]);
#pragma unroll
        for (int st_ = 1; st_ < 4; ++st_) K[pt][st_] = cmul(K[pt][st_ - 1], lam[pt]);
#pragma unroll
        for (int st_ = 0; st_ < 4; ++st_) {
            Or_[pt][st_] = fma2(splat2(cf2[pt].r), Lr[pt][st_], splat2(-cf2[pt].i) * Li[pt][st_]);
            Oi_[pt][st_] = fma2(splat2(-cf2[pt].r), Li[pt][st_], splat2(-cf2[pt].i) * Lr[pt][st_]);
        }
    }
#pragma unroll
    for (int pt = 0; pt < 3; ++pt) {
        asm volatile("" : "+v"(A2r[pt]), "+v"(A2i[pt]), "+v"(B2r[pt]), "+v"(B2i[pt]));
#pragma unroll
        for (int st_ = 0; st_ < 4; ++st_) { asm volatile("" : "+v"(Or_[pt][st_]), "+v"(Oi_[pt][st_])); PIN2(K[pt][st_]); }
    }
    STAMP(5);
    BAR();
    STAMP(10);
    {
        const __amdgpu_buffer_rsrc_t rh = mkrsrc(p.ws + WS_HT, 32 * 1152 * 128);
        const unsigned hoff = ((2 * c + (w >> 1)) * 1152 + 48 * g + pl) * 128 + (32 * (w & 1) + 8 * q) * 2;
        f32x2 cr[3], ci[3];
#pragma unroll
        for (int pt = 0; pt < 3; ++pt) {
            const f32x2 h0 = *(LAS f32x2*)(lds + M_XP + (pt * 16 + pl) * 8), h1 = *(LAS f32x2*)(lds + M_XP + 384 + (pt * 16 + pl) * 8);
            const f32x2 Hr = splat2(h0.x + h1.x), Hi = splat2(h0.y + h1.y);
            cr[pt] = fma2(A2r[pt], Hr, fma2(-A2i[pt], Hi, B2r[pt])); ci[pt] = fma2(A2r[pt], Hi, fma2(A2i[pt], Hr, B2i[pt]));
        }
#pragma unroll
        for (int pt = 0; pt < 3; ++pt) {
            f32x2 orr[4], oi[4];
#pragma unroll
            for (int st_ = 0; st_ < 4; ++st_) {
                orr[st_] = fma2(splat2(K[pt][st_].r), cr[pt], fma2(splat2(-K[pt][st_].i), ci[pt], Or_[pt][st_]));
                oi[st_] = fma2(splat2(-K[pt][st_].r), ci[pt], fma2(splat2(-K[pt][st_].i), cr[pt], Oi_[pt][st_]));
            }
            const u32x4 vr = {pk2(orr[0].x, orr[1].x), pk2(orr[2].x, orr[3].x), pk2(orr[0].y, orr[1].y), pk2(orr[2].y, orr[3].y)};
            const u32x4 vi = {pk2(oi[0].x, oi[1].x), pk2(oi[2].x, oi[3].x), pk2(oi[0].y, oi[1].y), pk2(oi[2].y, oi[3].y)};
            __builtin_amdgcn_raw_buffer_store_b128(vr, rh, hoff + pt * 16 * 128, 0, 16);
            __builtin_amdgcn_raw_buffer_store_b128(vi, rh, hoff + (576 + pt * 16) * 128, 0, 16);
        }
    }
    STAMP(11);
    f32x4 ac2[3][2];
#pragma unroll
    for (int a = 0; a < 3; ++a)
#pragma unroll
        for (int lt = 0; lt < 2; ++lt) {
            const unsigned x0 = xs[lt][a][0], x1 = xs[lt][a][1], d0 = dsk[a][0], d1 = dsk[a][1];
            ac2[a][lt][0] = rbf(__uint_as_float(x0 << 16) * __uint_as_float(d0 << 16));
            ac2[a][lt][1] = rbf(__uint_as_float(x0 & 0xffff0000u) * __uint_as_float(d0 & 0xffff0000u));
            ac2[a][lt][2] = rbf(__uint_as_float(x1 << 16) * __uint_as_float(d1 << 16));
            ac2[a][lt][3] = rbf(__uint_as_float(x1 & 0xffff0000u) * __uint_as_float(d1 & 0xffff0000u));
            asm volatile("" : "+v"(ac2[a][lt]));
        }
    WAITV(0);
    BAR();
    if (w == 0 && lane == 0) __hip_atomic_store((GAS unsigned*)(p.ws + WS_FLAG) + (c * 12 + g) * 32, tag, __ATOMIC_RELAXED, __HIP_MEMORY_SCOPE_AGENT);
    STAMP(6);
    unsigned coff[2], hoffs[2][2];
#pragma unroll
    for (int kk = 0; kk < 2; ++kk) coff[kk] = (48 * dh + pl) * 128 + (((4 * kk + q) ^ ((pl >> 1) & 7)) << 4);
    {
        const int a = pl >> 2, b = pl & 3;
#pragma unroll
        for (int hh = 0; hh < 2; ++hh) {
            const int k = 8 * q + 4 * hh + a;
            const int f = ((k >> 1) & 1) | (((k >> 3) & 1) << 1);
#pragma unroll
            for (int lt = 0; lt < 2; ++lt) { const int ch = 2 * (2 * lh + lt) + (b >> 1);
                hoffs[lt][hh] = G2_H0 + k * 128 + ((ch ^ (f << 1)) << 4) + (b & 1) * 8; }
        }
    }
    bf16x8 fc[2][2][3], fh[2][2][2];
    auto g2_read = [&](int bf, int cslot, int hslot) {
        LAS char* cs = lds + cslot * G2_CS; LAS char* hs = lds + hslot * G2_HS;
#pragma unroll
        for (int kk = 0; kk < 2; ++kk) {
#pragma unroll
            for (int lt = 0; lt < 2; ++lt) {
                const s16x4 lo = __builtin_amdgcn_ds_read_tr16_b64_v4i16((LAS s16x4*)(hs + kk * 4096 + hoffs[lt][0]));
                const s16x4 hi = __builtin_amdgcn_ds_read_tr16_b64_v4i16((LAS s16x4*)(hs + kk * 4096 + hoffs[lt][1]));
                fh[bf][kk][lt] = __builtin_shufflevector(lo, hi, 0, 1, 2, 3, 4, 5, 6, 7);
            }
#pragma unroll
            for (int a = 0; a < 3; ++a) fc[bf][kk][a] = *(LAS bf16x8*)(cs + coff[kk] + a * 2048);
        }
    };
    auto g2_mma = [&](int bf) {
#pragma unroll
        for (int kk = 0; kk < 2; ++kk)
#pragma unroll
            for (int a = 0; a < 3; ++a)
#pragma unroll
                for (int lt = 0; lt < 2; ++lt) ac2[a][lt] = mfma16(fc[bf][kk][a], fh[bf][kk][lt], ac2[a][lt]);
    };
    auto g2_sched = [&]() {
        SGB(0x8, 1); SGB(0x100, 2); SGB(0x8, 1); SGB(0x100, 2);
#pragma unroll
        for (int i = 0; i < 10; ++i) { SGB(0x8, 1); SGB(0x100, 1); }
    };
    SBAR();
    STAMP(7);
    g2_read(0, 0, 0);
    int cs1 = 1, hs1 = 1;
#pragma unroll 1
    for (int s = 0; s < 16; s += 2) {
        SBAR();
        g2_read(1, cs1, hs1); g2_mma(0); g2_sched();
        cs1 = cs1 == G2_NC - 1 ? 0 : cs1 + 1; hs1 = hs1 == G2_NH - 1 ? 0 : hs1 + 1;
        SBAR();
        g2_read(0, cs1, hs1); g2_mma(1); g2_sched();
        cs1 = cs1 == G2_NC - 1 ? 0 : cs1 + 1; hs1 = hs1 == G2_NH - 1 ? 0 : hs1 + 1;
    }
    SBAR();
    g2_read(1, cs1, hs1); g2_mma(0); g2_sched();
    SCHED();
    g2_mma(1);
    SCHED();
    STAMP(8);
#pragma unroll
    for (int a = 0; a < 3; ++a)
#pragma unroll
        for (int lt = 0; lt < 2; ++lt)
            __builtin_amdgcn_raw_buffer_store_b128(__builtin_bit_cast(u32x4, ac2[a][lt]), mkrsrc(p.out, L * OUTW * 4), ((64 * hc + 32 * lh + 16 * lt + pl) * OUTW + D + 96 * dg + 48 * dh + 16 * a + 4 * q) * 4, 0, OUT_AUX);
    STAMP(9); CSTAMP(15);
    if (w == 0) STAMP_OUT(0);
}

template <int TM> DEVI void a_conv(LAS char* lds, int ch, int lane, f32x4 (&acc)[8]) {
    constexpr int TMAX = TM & 128 ? 7 : TM & 64 ? 6 : TM & 32 ? 5 : 4;
    static_assert(TM >= 16, "");
    const int fi = lane & 15, gq = lane >> 4, par = fi & 1, hp = fi >> 1;
    LAS char* kp = lds + ch * A_KSZ + par * A_KB + 2 * (2044 - 4 * hp + 8 * gq) - 512 * 7;
    LAS char* ub = lds + A_U0 + ch * A_USZ + (par ? 0 : A_UC);
    bf16x8 fa[2][8], fu[2][8];
    auto rd = [&](int bf, int nu) {
        LAS char* k = kp - 64 * nu;
        const int R = hp - nu + 7;
        const unsigned un = (2 * gq) ^ (((R >> 2) & 1) << 2) ^ (par ? 0 : 1);
        LAS char* u0 = ub + 64 * R + 8 * un; LAS char* u1 = ub + 64 * R + 8 * (un ^ 1);
#pragma unroll
        for (int m = 0; m <= TMAX; ++m) {
            const u32x2 alo = *(volatile LAS u32x2*)(k + 512 * (7 - m)), ahi = *(volatile LAS u32x2*)(k + 512 * (7 - m) + 8);
            fa[bf][m] = __builtin_bit_cast(bf16x8, (u32x4){alo[0], alo[1], ahi[0], ahi[1]});
            bool need = false;
#pragma unroll
            for (int t = m; t < 8; ++t) need |= ((TM >> t) & 1) != 0;
            const u32x2 blo = *(volatile LAS u32x2*)(u0 + 512 * m), bhi = *(volatile LAS u32x2*)(u1 + 512 * m);
            fu[bf][m] = __builtin_bit_cast(bf16x8, (u32x4){blo[0], blo[1], bhi[0], bhi[1]});
        }
    };
    auto mm = [&](int bf) {
#pragma unroll
        for (int dg_ = 0; dg_ <= 2 * TMAX; ++dg_)
#pragma unroll
            for (int m = 0; m <= TMAX; ++m) {
                const int kp_ = dg_ - m;
                if (kp_ < 0 || kp_ > TMAX || m + kp_ > 7) continue;
                if (m > kp_ && false) continue;
                const int t = m + kp_;
                if (t == dg_ && ((TM >> t) & 1)) acc[t] = mfma16(fa[bf][m], fu[bf][kp_], acc[t]);
            }
    };
    auto sched = [&]() {
        constexpr int NM = [] { int n = 0; for (int t = 0; t < 8; ++t) if ((TM >> t) & 1) n += t + 1; return n; }(), NR = 4 * (TMAX + 1);
        constexpr int PER = (NR + NM - 1) / NM;
#pragma unroll
        for (int i = 0; i < NM; ++i) { SGB(0x8, 1); SGB(0x100, PER); }
    };
    rd(0, 0);
#pragma unroll 1
    for (int nu = 0; nu < 8; nu += 2) {
        SCHED();
        rd(1, nu + 1); mm(0); sched();
        SCHED();
        rd(0, nu + 2 < 8 ? nu + 2 : 7); mm(1); sched();
    }
    SCHED();
}

template <int TM> DEVI void a_finish(LAS char* lds, int ch, int lane, float dskip, const f32x4 (&acc)[8], LAS char* yrow) {
    const int fi = lane & 15, gq = lane >> 4;
    const int t0 = 32 * (fi >> 1) + 2 * (fi & 1) + 8 * gq;
    LAS char* y = yrow + 2 * t0;
    LAS char* ua[2];
#pragma unroll
    for (int hh = 0; hh < 2; ++hh) { const int e = t0 + 4 * hh + 224, R = e >> 5, un = ((e >> 2) & 7) ^ (((R >> 2) & 1) << 2);
        ua[hh] = lds + A_U0 + ch * A_USZ + 64 * R + 8 * un + 2 * (e & 3); }
    unsigned uu[8][2];
#pragma unroll
    for (int t = 0; t < 8; ++t)
        if ((TM >> t) & 1) { uu[t][0] = *(LAS unsigned*)(ua[0] + 512 * t); uu[t][1] = *(LAS unsigned*)(ua[1] + 512 * t); }
#pragma unroll
    for (int t = 0; t < 8; ++t) {
        if (!((TM >> t) & 1)) continue;
#pragma unroll
        for (int hh = 0; hh < 2; ++hh) {
            const float s0 = rbf(__uint_as_float(uu[t][hh] << 16) * dskip), s1 = rbf(__uint_as_float(uu[t][hh] & 0xffff0000u) * dskip);
            *(LAS unsigned*)(y + 512 * t + 8 * hh) = pk2(rbf(acc[t][2 * hh]) + s0, rbf(acc[t][2 * hh + 1]) + s1);
        }
    }
}

DEVI void a_block(const Params& p, LAS char* lds, int xl, int ja, int wave, int lane, unsigned cpf) {
    STAMP_DECL;
    STAMP(0); CSTAMP(14);
    const int tid = threadIdx.x, d0 = 72 * xl + 9 * ja;
    typedef __attribute__((ext_vector_type(3))) unsigned u32x3;
    const u16 dskr = p.ADskip[d0 + wave], dsk8r = p.ADskip[d0 + 8];
    if (wave < 4) {
        const AParam pq = a_params(p, d0 + wave, lane);
        if (tid == 0) *(LAS unsigned*)(lds + A_FLAG) = 0;
        a_ktable(pq, lds, wave * A_KSZ, lane);
        a_expand(lds + wave * A_KSZ, lane);
        asm volatile("" :: "v"(cpf));
        STAMP(1);
        BAR();
    } else {
        const int t4 = tid - 256;
        const unsigned eraw = __hip_atomic_load((GAS unsigned*)(p.ws + WS_E), __ATOMIC_RELAXED, __HIP_MEMORY_SCOPE_AGENT);
        const __amdgpu_buffer_rsrc_t ru = mkrsrc(p.Au, L * D * 2);
        const int wst = min((2 * d0) & ~3, D * 2 - 24), sh = (2 * d0 - wst) >> 1, half = t4 & 1;
        u32x3 uv[4][4];
        auto uload = [&](int tk) {
            const int grp = (tk * 128 + (t4 >> 1) + 64 * ja) & 511;
#pragma unroll
            for (int i = 0; i < 4; ++i) uv[tk][i] = __builtin_amdgcn_raw_buffer_load_b96(ru, (4 * grp + i) * (D * 2) + wst + 12 * half, 0, 0);
        };
        int ubase[6];
#pragma unroll
        for (int e = 0; e < 6; ++e) { const int ch = 6 * half + e - sh; ubase[e] = (ch >= 0 && ch < 9) ? A_U0 + ch * A_USZ : -1; }
        auto ustage = [&](int tk) {
            const int grp = (tk * 128 + (t4 >> 1) + 64 * ja) & 511;
            const int un = grp + 56, R = un >> 3, sw = ((R >> 2) & 1) << 2, un1 = un + 1, R1 = un1 >> 3, sw1 = ((R1 >> 2) & 1) << 2;
            const int a0 = 64 * R + 8 * ((un & 7) ^ sw), a1 = A_UC + 64 * R + 8 * ((un & 7) ^ sw ^ 1) + 4, a2 = A_UC + 64 * R1 + 8 * ((un1 & 7) ^ sw1 ^ 1);
#pragma unroll
            for (int e = 0; e < 6; ++e) {
                const unsigned sel = (e & 1) ? 0x07060302u : 0x05040100u;
                const unsigned lo = __builtin_amdgcn_perm(uv[tk][1][e >> 1], uv[tk][0][e >> 1], sel), hi = __builtin_amdgcn_perm(uv[tk][3][e >> 1], uv[tk][2][e >> 1], sel);
                const bool ok = ubase[e] >= 0;
                *(LAS u32x2*)(lds + (ok ? ubase[e] + a0 : A_DUMMY + 8 * lane)) = (u32x2){lo, hi};
                *(LAS unsigned*)(lds + (ok ? ubase[e] + a1 : A_DUMMY + 8 * lane)) = lo;
                *(LAS unsigned*)(lds + (ok ? ubase[e] + a2 : A_DUMMY + 8 * lane)) = hi;
            }
        };
        uload(0); uload(1);
        SCHED();
        {
#pragma unroll
            for (int k = 0; k < 3; ++k) {
                const int ch = (wave - 4) + 4 * k;
                if (ch < 9) {
                    LAS char* ub = lds + A_U0 + ch * A_USZ;
                    if (lane < 56) { *(LAS u32x2*)(ub + 8 * lane) = (u32x2){0u, 0u}; *(LAS u32x2*)(ub + A_UC + 8 * lane) = (u32x2){0u, 0u}; }
                    else if (lane == 56) *(LAS unsigned*)(ub + A_UC + 64 * 7 + 8 * (0 ^ 4 ^ 1)) = 0u;
                }
            }
        }
        SCHED();
        uload(2); SCHED(); ustage(0); SCHED();
        uload(3); SCHED(); ustage(1); SCHED();
        ustage(2); ustage(3);
        const unsigned tag = next_tag(__builtin_amdgcn_readfirstlane(eraw));
        if (tid == 256) __hip_atomic_store((GAS unsigned*)(p.ws + WS_AFLAG) + (xl * 8 + ja) * 32, tag, __ATOMIC_RELAXED, __HIP_MEMORY_SCOPE_AGENT);
        asm volatile("" :: "v"(cpf));
        STAMP(1);
        BAR();
        STAMP(2);
        unsigned warm[3];
#pragma unroll
        for (int k = 0; k < 3; ++k) {
            const int piece = (ja * 4 + (wave - 4)) * 3 + k;
            warm[k] = ((const GAS unsigned*)((piece & 1) ? p.BCim : p.BCre))[((piece >> 1) * 3 * 4096 + lane * 64) / 4];
        }
        const __amdgpu_buffer_rsrc_t rk = mkrsrc(p.ws + WS_KT, 576 * KT_STRIDE);
#pragma unroll 1
        for (int pass = 0; pass < (wave == 4 ? 2 : 1); ++pass) {
            const int ch = pass ? 8 : wave;
            u32x4 v[3];
            for (unsigned spins = 0; spins < SPIN_CAP; ++spins) {
                bool ok = true;
#pragma unroll
                for (int i = 0; i < 3; ++i) { v[i] = __builtin_amdgcn_raw_buffer_load_b128(rk, (d0 + ch) * KT_STRIDE + 16 * min(64 * i + lane, 160), 0, 16); ok &= (v[i][1] == tag) & (v[i][3] == tag); }
                if (__all(ok)) break;
                __builtin_amdgcn_s_sleep(2);
                asm volatile("" ::: "memory");
            }
            LAS char* kreg = lds + ch * A_KSZ;
#pragma unroll
            for (int i = 0; i < 3; ++i) *(LAS u32x2*)(kreg + 8 * min(64 * i + lane, 160)) = (u32x2){v[i][0], v[i][2]};
            asm volatile("" ::: "memory");
            a_expand(kreg, lane);
            if (pass) { asm volatile("s_waitcnt lgkmcnt(0)" ::: "memory"); if (lane == 0) *(volatile LAS unsigned*)(lds + A_FLAG) = 1u; }
        }
        asm volatile("" :: "v"(warm[0]), "v"(warm[1]), "v"(warm[2]));
    }
    STAMP(3);
    f32x4 acc[8];
#pragma unroll
    for (int t = 0; t < 8; ++t) acc[t] = (f32x4){0, 0, 0, 0};
    a_conv<0xff>(lds, wave, lane, acc);
    STAMP(4);
    a_finish<0xff>(lds, wave, lane, bf2f(dskr), acc, lds + wave * A_KSZ + wave * 16);
    STAMP(5);
    if (wave < 4) {
        for (unsigned spins = 0; spins < SPIN_CAP && *(volatile LAS unsigned*)(lds + A_FLAG) == 0u; ++spins) __builtin_amdgcn_s_sleep(1);
        asm volatile("" ::: "memory");
#pragma unroll
        for (int t = 0; t < 8; ++t) acc[t] = (f32x4){0, 0, 0, 0};
        const float dsk8 = bf2f(dsk8r);
        LAS char* y8 = lds + wave * A_KSZ + A_Y8;
        if (wave == 0) { a_conv<0x81>(lds, 8, lane, acc); a_finish<0x81>(lds, 8, lane, dsk8, acc, y8); }
        else if (wave == 1) { a_conv<0x42>(lds, 8, lane, acc); a_finish<0x42>(lds, 8, lane, dsk8, acc, y8); }
        else if (wave == 2) { a_conv<0x24>(lds, 8, lane, acc); a_finish<0x24>(lds, 8, lane, dsk8, acc, y8); }
        else { a_conv<0x18>(lds, 8, lane, acc); a_finish<0x18>(lds, 8, lane, dsk8, acc, y8); }
    }
    STAMP(6);
    BAR();
    STAMP(7);
    const __amdgpu_buffer_rsrc_t ro = mkrsrc(p.out, L * OUTW * 4);
#pragma unroll
    for (int r = 0; r < 12; ++r) {
        const int id = r * 512 + tid, row = id / 3, th = id % 3, tau = row >> 8;
        u32x3 v;
#pragma unroll
        for (int i = 0; i < 3; ++i) {
            const int ch = 3 * th + i;
            const int base = ch < 8 ? ch * A_KSZ + ch * 16 : (tau < 4 ? tau : 7 - tau) * A_KSZ + A_Y8;
            v[i] = (unsigned)*(LAS u16*)(lds + base + 2 * row) << 16;
        }
        __builtin_amdgcn_raw_buffer_store_b96(v, ro, (row * OUTW + d0 + 3 * th) * 4, 0, 0);
    }
    STAMP(8); CSTAMP(15); STAMP(9);
    if (wave == 0) STAMP_OUT(0);
    if (wave == 4) STAMP_OUT(16);
}

DEVI void closer(const Params& p) {
    const int lane = threadIdx.x & 63;
    GAS unsigned* Ew = (GAS unsigned*)(p.ws + WS_E);
    const unsigned tag = next_tag(__builtin_amdgcn_readfirstlane(__hip_atomic_load(Ew, __ATOMIC_RELAXED, __HIP_MEMORY_SCOPE_AGENT)));
    GAS unsigned* fl = (GAS unsigned*)(p.ws + WS_FLAG);
    for (unsigned spins = 0; spins < SPIN_CAP; ++spins) {
        bool ok = true;
#pragma unroll
        for (int i = 0; i < 4; ++i) ok &= __hip_atomic_load(fl + (lane + 64 * i) * 32, __ATOMIC_RELAXED, __HIP_MEMORY_SCOPE_AGENT) == tag;
        if (__all(ok)) break;
        __builtin_amdgcn_s_sleep(8);
    }
    if (lane == 0) __hip_atomic_store(Ew, tag, __ATOMIC_RELAXED, __HIP_MEMORY_SCOPE_AGENT);
}

__global__ __launch_bounds__(512, 2) void fused_kernel(Params p) {
    extern __shared__ __attribute__((aligned(16))) char smem[];
    LAS char* lds = (LAS char*)smem;
    const int b = blockIdx.x, xl = b & 7, j = b >> 3;
    asm volatile("" :: "s"(p.Bx), "s"(p.BBre), "s"(p.BBim), "s"(p.BCre), "s"(p.BCim), "s"(p.ws), "s"(p.out), "s"(p.BDskip), "s"(p.BLre), "s"(p.BLim), "s"(p.Blogdt));
    asm volatile("" :: "s"(p.Au), "s"(p.AlogA), "s"(p.Aim), "s"(p.ACre), "s"(p.ACim), "s"(p.Alogdt), "s"(p.ADskip));
    const int wave = __builtin_amdgcn_readfirstlane(threadIdx.x >> 6), lane = threadIdx.x & 63;
    unsigned long long pc; asm volatile("s_getpc_b64 %0" : "=s"(pc));
    const unsigned cpf = *(const GAS unsigned*)((const GAS char*)(pc & ~63ull) + ((j + wave * 4) % CODE_SLICES) * 4096 + lane * 64);
    if (j < 24) { if (wave >= 4) b_loader(p, lds, xl, j, wave, lane, cpf); else b_compute(p, lds, xl, j, wave, lane, cpf); }
    else { a_block(p, lds, xl, j - 24, wave, lane, cpf); if (b == 255 && wave == 7) closer(p); }
}

extern "C" void kernel_launch(void* const* d_in, const int* in_sizes, int n_in, void* d_out, int out_size, void* d_ws, size_t ws_size, hipStream_t stream) {
    Params p;
    const u16* const* in = (const u16* const*)d_in;
    p.Au = in[0]; p.AlogA = in[1]; p.Aim = in[2]; p.ACre = in[3]; p.ACim = in[4]; p.Alogdt = in[5]; p.ADskip = in[6];
    p.Bx = in[7]; p.BLre = in[8]; p.BLim = in[9]; p.BBre = in[10]; p.BBim = in[11]; p.BCre = in[12]; p.BCim = in[13]; p.Blogdt = in[14]; p.BDskip = in[15];
    p.out = (float*)d_out; p.ws = (char*)d_ws;
    (void)hipFuncSetAttribute((const void*)fused_kernel, hipFuncAttributeMaxDynamicSharedMemorySize, LDS_BYTES);
    fused_kernel<<<256, 512, LDS_BYTES, stream>>>(p);
}
```
